```python
import torch

def _softmax(x, axis=-1):
    return torch.softmax(x, dim=axis)

def _gelu(x):
    return torch.nn.functional.gelu(x, approximate='tanh')

def _silu(x):
    return torch.nn.functional.silu(x)

def _sigmoid(x):
    return torch.sigmoid(x)

def _layernorm(x, axis=-1, eps=1e-05):
    m = x.mean(dim=axis, keepdim=True)
    v = x.var(dim=axis, keepdim=True, unbiased=False)
    return (x - m) / torch.sqrt(v + eps)

def _scatter_add(tgt, idx, src, axis=0):
    return tgt.clone().index_add_(axis, idx, src)

def _segment_sum(src, seg, n):
    out = torch.zeros((n,) + src.shape[1:], dtype=src.dtype)
    return out.index_add_(0, seg, src)

def _pad(x, pw):
    flat = tuple((v for pair in reversed(pw) for v in pair))
    return torch.nn.functional.pad(x, flat)
kernel_name = 'adaLN_cond__ssm_conv_h3__glu_gated_bilinear__prompt'

def setup_inputs():

    _w = lambda a: (lambda t: t.to(torch.bfloat16) if t.is_floating_point() else t)(torch.as_tensor(a))

    def _A_setup():
        B, S, D = (1, 2048, 512)
        ks = 4
        return {'q': _w((lambda *s: torch.randn(*s, dtype=torch.bfloat16) * 0.05)(B, S, D)), 'k': _w((lambda *s: torch.randn(*s, dtype=torch.bfloat16) * 0.8)(B, S, D)), 'v': _w((lambda *s: torch.randn(*s, dtype=torch.bfloat16) * 0.8)(B, S, D)), 'shift_k': _w((lambda *s: torch.randn(*s, dtype=torch.bfloat16) * 0.8)(D, ks) * 0.5), 'ssm_k': _w((lambda *s: torch.randn(*s, dtype=torch.bfloat16) * 0.8)(D, S) * (1.0 / S ** 0.5)), 'ssm_D': _w((lambda *s: torch.randn(*s, dtype=torch.bfloat16) * 0.8)(D))}
    _A_in = _A_setup()

    def _B_setup():
        B, S, D, F = (1, 2048, 512, 1792)
        return {'x': _w((lambda *s: torch.randn(*s, dtype=torch.bfloat16) * 0.05)(B, S, D)), 'w_gate': _w((lambda *s: torch.randn(*s, dtype=torch.bfloat16) * 0.8)(D, F) * D ** (-0.5)), 'w_up': _w((lambda *s: torch.randn(*s, dtype=torch.bfloat16) * 0.8)(D, F) * D ** (-0.5)), 'w_down': _w((lambda *s: torch.randn(*s, dtype=torch.bfloat16) * 0.8)(F, D) * F ** (-0.5))}
    _B_in = _B_setup()
    return {'A_q': _A_in['q'], 'A_k': _A_in['k'], 'A_v': _A_in['v'], 'A_shift_k': _A_in['shift_k'], 'A_ssm_k': _A_in['ssm_k'], 'A_ssm_D': _A_in['ssm_D'], 'B_x': _B_in['x'], 'B_w_gate': _B_in['w_gate'], 'B_w_up': _B_in['w_up'], 'B_w_down': _B_in['w_down'], 'c': _w((lambda *s: torch.randn(*s, dtype=torch.bfloat16) * 0.2)(1, 512) * 8.0), 'w_mod': _w((lambda *s: torch.randn(*s, dtype=torch.bfloat16) * 0.2)(512, 6 * 512) * 512 ** (-0.5) * 8.0)}

def reference(A_q, A_k, A_v, A_shift_k, A_ssm_k, A_ssm_D, B_x, B_w_gate, B_w_up, B_w_down, c, w_mod):

    def _A_ref(q, k, v, shift_k, ssm_k, ssm_D):
        B, S, D = q.shape
        ks = shift_k.shape[-1]
        k_pad = _pad(k, ((0, 0), (ks - 1, 0), (0, 0)))
        k_shift = torch.zeros_like(k)
        for t in range(ks):
            k_shift = k_shift + k_pad[:, t:t + S, :] * shift_k[:, ks - 1 - t][None, None, :]
        kv = k_shift * v
        L = 2 * S
        kv_pad = _pad(kv, ((0, 0), (0, S), (0, 0)))
        kf_pad = _pad(ssm_k, ((0, 0), (0, S)))
        kv_f = (lambda x, axes=(-2, -1): torch.fft.rfft2(x.float(), dim=axes))(kv_pad[:, None, :, :], axes=(1, 2))
        kf_f = (lambda x, axes=(-2, -1): torch.fft.rfft2(x.float(), dim=axes))(kf_pad[:, None, :], axes=(1, 2))
        kf_f = (lambda x, *d: x.permute(*d))(kf_f, 1, 2, 0)
        y_f = kv_f * kf_f[None, :, :, :]
        y = (lambda x, s, axes=(-2, -1): torch.fft.irfft2(x, s=s, dim=axes).to(torch.bfloat16))(y_f, (1, L), axes=(1, 2))[:, 0, :S, :]
        y = y + kv * ssm_D[None, None, :]
        return q * y

    def _B_ref(x, w_gate, w_up, w_down):
        g = torch.einsum('bsd,df->bsf', x, w_gate)
        u = torch.einsum('bsd,df->bsf', x, w_up)
        h = g * u
        return torch.einsum('bsf,fd->bsd', h, w_down)
    x = A_q
    mod = (_silu(c) @ w_mod).reshape(c.shape[0], 6, 1, -1)
    sa, ca, ga, sb, cb, gb = (mod[:, 0].clone(), mod[:, 1].clone(), mod[:, 2].clone(), mod[:, 3].clone(), mod[:, 4].clone(), mod[:, 5].clone())
    h = _layernorm(x, axis=-1) * (1 + ca) + sa
    x = x + ga * _A_ref(h, A_k, A_v, A_shift_k, A_ssm_k, A_ssm_D)
    h = _layernorm(x, axis=-1) * (1 + cb) + sb
    x = x + gb * _B_ref(h, B_w_gate, B_w_up, B_w_down)
    return x
```

```cpp
#include <hip/hip_runtime.h>
#include <hip/hip_bf16.h>
typedef unsigned short u16;
__device__ __forceinline__ float b2f(u16 x){ return __uint_as_float(((unsigned)x)<<16); }
__device__ __forceinline__ u16 f2b(float f){ __hip_bfloat16 b = __float2bfloat16(f); return *(u16*)&b; }
__device__ __forceinline__ float rb(float f){ return b2f(f2b(f)); }
constexpr int S=2048, D=512, F=1792;

__global__ void k_mod(const u16* c, const u16* w, float* mod){
  int n = blockIdx.x*blockDim.x+threadIdx.x; if(n>=6*D) return;
  float acc=0; for(int k=0;k<D;k++){ float x=b2f(c[k]); float s=rb(x/(1.f+__expf(-x))); acc+=s*b2f(w[k*6*D+n]); }
  mod[n]=rb(acc);
}
__global__ void k_kv(const u16* k,const u16* v,const u16* sh,u16* kv){
  int i=blockIdx.x*blockDim.x+threadIdx.x; int s=i/D,d=i%D;
  float a=0; for(int j=3;j>=0;j--){ float kk=(s-j>=0)?b2f(k[(s-j)*D+d]):0.f; a=rb(a+rb(kk*b2f(sh[d*4+j]))); }
  kv[i]=f2b(a*b2f(v[i]));
}
__global__ void k_conv(const u16* kv,const u16* sk,const u16* Dv,u16* y){
  int i=blockIdx.x*blockDim.x+threadIdx.x; int s=i/D,d=i%D;
  float a=0; for(int j=0;j<=s;j++) a+=b2f(sk[d*S+j])*b2f(kv[(s-j)*D+d]);
  y[i]=f2b(rb(a)+rb(b2f(kv[i])*b2f(Dv[d])));
}
__device__ float bsum(float v,float* sm){ __syncthreads(); sm[threadIdx.x]=v; __syncthreads(); for(int o=256;o>0;o>>=1){ if(threadIdx.x<o) sm[threadIdx.x]+=sm[threadIdx.x+o]; __syncthreads(); } return sm[0]; }
__global__ void k_row(const u16* q,const u16* y,const float* mod,u16* x1,u16* h2){
  __shared__ float sm[512]; int s=blockIdx.x,d=threadIdx.x;
  float x=b2f(q[s*D+d]); float m=bsum(x,sm)/D; float vv=bsum((x-m)*(x-m),sm)/D;
  float ln=rb(rb(x-rb(m))/rb(sqrtf(rb(rb(vv)+1e-5f))));
  float h=rb(rb(ln*rb(1.f+mod[D+d]))+mod[d]);
  float xx=rb(x+rb(mod[2*D+d]*rb(h*b2f(y[s*D+d]))));
  m=bsum(xx,sm)/D; vv=bsum((xx-m)*(xx-m),sm)/D;
  ln=rb(rb(xx-rb(m))/rb(sqrtf(rb(rb(vv)+1e-5f))));
  h=rb(rb(ln*rb(1.f+mod[4*D+d]))+mod[3*D+d]);
  x1[s*D+d]=f2b(xx); h2[s*D+d]=f2b(h);
}
__global__ void k_g1(const u16* h2,const u16* wg,const u16* wu,u16* hh){
  int i=blockIdx.x*blockDim.x+threadIdx.x; int s=i/F,f=i%F; float g=0,u=0;
  for(int k=0;k<D;k++){ float a=b2f(h2[s*D+k]); g+=a*b2f(wg[k*F+f]); u+=a*b2f(wu[k*F+f]); }
  hh[i]=f2b(rb(g)*rb(u));
}
__global__ void k_g2(const u16* hh,const u16* wd,const u16* x1,const float* mod,u16* out){
  int i=blockIdx.x*blockDim.x+threadIdx.x; int s=i/D,d=i%D; float a=0;
  for(int k=0;k<F;k++) a+=b2f(hh[s*F+k])*b2f(wd[k*D+d]);
  out[i]=f2b(b2f(x1[i])+rb(mod[5*D+d]*rb(a)));
}
extern "C" void kernel_launch(void* const* d_in, const int* in_sizes, int n_in, void* d_out, int out_size, void* d_ws, size_t ws_size, hipStream_t stream){
  const u16 *q=(const u16*)d_in[0],*k=(const u16*)d_in[1],*v=(const u16*)d_in[2],*sh=(const u16*)d_in[3],*sk=(const u16*)d_in[4],*Dv=(const u16*)d_in[5];
  const u16 *wg=(const u16*)d_in[7],*wu=(const u16*)d_in[8],*wd=(const u16*)d_in[9],*c=(const u16*)d_in[10],*wm=(const u16*)d_in[11];
  char* ws=(char*)d_ws; float* mod=(float*)ws; u16* kv=(u16*)(ws+(1<<20)); u16* y=(u16*)(ws+(4<<20)); u16* x1=(u16*)(ws+(8<<20)); u16* h2=(u16*)(ws+(12<<20)); u16* hh=(u16*)(ws+(16<<20));
  k_mod<<<12,256,0,stream>>>(c,wm,mod);
  k_kv<<<S*D/256,256,0,stream>>>(k,v,sh,kv);
  k_conv<<<S*D/256,256,0,stream>>>(kv,sk,Dv,y);
  k_row<<<S,512,0,stream>>>(q,y,mod,x1,h2);
  k_g1<<<S*F/256,256,0,stream>>>(h2,wg,wu,hh);
  k_g2<<<S*D/256,256,0,stream>>>(hh,wd,x1,mod,(u16*)d_out);
}
```

```cpp
#include <hip/hip_runtime.h>
typedef unsigned short u16; typedef unsigned u32; typedef unsigned long long u64;
typedef u32 v2u __attribute__((ext_vector_type(2)));
typedef u32 v4u __attribute__((ext_vector_type(4)));
typedef float v2f __attribute__((ext_vector_type(2)));
typedef float v4f __attribute__((ext_vector_type(4)));
typedef float v16f __attribute__((ext_vector_type(16)));
typedef short v4s __attribute__((ext_vector_type(4)));
typedef short v8s __attribute__((ext_vector_type(8)));
typedef __bf16 v2bf __attribute__((ext_vector_type(2)));
#define LAS __attribute__((address_space(3)))
#define GAS __attribute__((address_space(1)))
#define DI __device__ __forceinline__
#define SB() __builtin_amdgcn_sched_barrier(0)
#define RLX_AGENT __ATOMIC_RELAXED, __HIP_MEMORY_SCOPE_AGENT
#define FUSED_LAUNCH 1
#define STAMPS 0

constexpr int S = 2048, D = 512, F = 1792;
constexpr float EPS = 1e-5f;
constexpr u32 EPOCH_OFF = 0, FLAGA_OFF = 0x1000, CNT0_OFF = 0xA000, CNTB_OFF = 0xE000, FLAGS_OFF = 0x12000, FLAGC_OFF = 0x14000;
constexpr u32 ST1_OFF = 0x40000, ST2_OFF = 0x44000, MODP_OFF = 0x48000, TV_OFF = 0x58000, DBG_OFF = 0x80000;
constexpr u32 WT_OFF = 1u << 20, QT_OFF = 3u << 20, X1_OFF = 5u << 20, X1S_OFF = 7u << 20, WG_OFF = 9u << 20, WD_OFF = 13u << 20, HH_OFF = 15u << 20, WS_BYTES = 22u << 20;
constexpr u32 KC_L = 0, KC_CH = 33280, KC_CS = 4160;
constexpr u32 WL_L = 66560, WL_CH = 4608;
constexpr u32 RED_L = 75776;
constexpr u32 KV_L = 92160, QL_L = 100352, WS_L = 108544, WDS_L = 124928;
constexpr u32 A_L = 0;
constexpr u32 EPI_L = 133120;
constexpr u32 SIG_L = 141312;
constexpr u32 X1L_L = 143360, GB_L = 151552;
constexpr u32 MISC_L = 151808, STMP_L = 152064, LDS_BYTES = 153600;
constexpr u32 SPIN_CAP = 1u << 17;

struct Params { const u16 *q, *k, *v, *sh, *sk, *Dv, *wg, *wu, *wd, *c, *wm; u16* out; char* ws; };

typedef __amdgpu_buffer_rsrc_t rsrc_t;
DI rsrc_t mkrs(const void* p, u32 bytes) { return __builtin_amdgcn_make_buffer_rsrc((void*)p, 0, bytes, 0x00020000); }
DI v4u mksrd(const void* p, u32 bytes) { v4u r = {(u32)(size_t)p, (u32)((size_t)p >> 32), bytes, 0x00020000u}; return r; }
DI v4u ld16(rsrc_t r, u32 vo, u32 so = 0) { return __builtin_amdgcn_raw_buffer_load_b128(r, vo, so, 0); }
DI v4u ld16c(rsrc_t r, u32 vo, u32 so = 0) { return __builtin_amdgcn_raw_buffer_load_b128(r, vo, so, 16); }
DI u32 ld4c(rsrc_t r, u32 vo, u32 so = 0) { return __builtin_amdgcn_raw_buffer_load_b32(r, vo, so, 16); }
DI v2u ld8c(rsrc_t r, u32 vo, u32 so = 0) { return __builtin_amdgcn_raw_buffer_load_b64(r, vo, so, 16); }
DI void st16c(v4u v, rsrc_t r, u32 vo, u32 so = 0) { __builtin_amdgcn_raw_buffer_store_b128(v, r, vo, so, 16); asm volatile("s_nop 1" ::"v"(v) : "memory"); }
DI void st16(v4u v, rsrc_t r, u32 vo, u32 so = 0) { __builtin_amdgcn_raw_buffer_store_b128(v, r, vo, so, 0); asm volatile("s_nop 1" ::"v"(v) : "memory"); }
DI float lo16(u32 x) { return __builtin_bit_cast(float, x << 16); }
DI float hi16(u32 x) { return __builtin_bit_cast(float, x & 0xffff0000u); }
DI v2f unpk(u32 x) { v2f r = {lo16(x), hi16(x)}; return r; }
DI u32 pk(v2f v) { return __builtin_bit_cast(u32, __builtin_convertvector(v, v2bf)); }
DI u32 pk(float a, float b) { v2f v = {a, b}; return pk(v); }
DI float rbf(float a) { return lo16(pk(a, a)); }
DI v4s trr(u32 a) { return __builtin_amdgcn_ds_read_tr16_b64_v4i16((LAS v4s*)a); }
DI v4u cat(v4s a, v4s b) { return __builtin_bit_cast(v4u, __builtin_shufflevector(a, b, 0, 1, 2, 3, 4, 5, 6, 7)); }
DI v4u lds16(u32 a) { return *(LAS v4u*)a; }
DI void sts16(u32 a, v4u v) { *(LAS v4u*)a = v; }
template <int C> DI float dppadd(float x) { return x + __builtin_bit_cast(float, __builtin_amdgcn_update_dpp(0, __builtin_bit_cast(int, x), C, 0xf, 0xf, true)); }
DI float swap16add(float x) { u32 u = __builtin_bit_cast(u32, x); auto r = __builtin_amdgcn_permlane16_swap(u, u, false, false); return __builtin_bit_cast(float, (u32)r[0]) + __builtin_bit_cast(float, (u32)r[1]); }
DI float swap32add(float x) { u32 u = __builtin_bit_cast(u32, x); auto r = __builtin_amdgcn_permlane32_swap(u, u, false, false); return __builtin_bit_cast(float, (u32)r[0]) + __builtin_bit_cast(float, (u32)r[1]); }
DI float wsum64(float x) { x = dppadd<0xB1>(x); x = dppadd<0x4E>(x); x = dppadd<0x141>(x); x = dppadd<0x140>(x); x = swap16add(x); return swap32add(x); }
DI float wsum_hi3(float x) { x = dppadd<0x128>(x); x = swap16add(x); return swap32add(x); }
#define DRAIN() asm volatile("s_waitcnt vmcnt(0)" ::: "memory")
#define WAITV(n) asm volatile("s_waitcnt vmcnt(%0)" ::"i"(n) : "memory")
#define BAR() asm volatile("s_waitcnt lgkmcnt(0)\n\ts_barrier" ::: "memory")
#define ORDER() __builtin_amdgcn_fence(__ATOMIC_ACQUIRE, "wavefront")
DI void glds(v4u srd, u32 vo, u32 so, u32 ldst) { asm volatile("s_mov_b32 m0, %0\n\ts_nop 0\n\tbuffer_load_dwordx4 %1, %2, %3 offen lds" ::"s"(ldst), "v"(vo), "s"(srd), "s"(so) : "memory"); }
DI void gldsc(v4u srd, u32 vo, u32 so, u32 ldst) { asm volatile("s_mov_b32 m0, %0\n\ts_nop 0\n\tbuffer_load_dwordx4 %1, %2, %3 offen sc1 lds" ::"s"(ldst), "v"(vo), "s"(srd), "s"(so) : "memory"); }
template <int N, int IMM> DI void lda(v4u srd, u32 vo, u32 so) { asm volatile("buffer_load_dwordx4 a[%c3:%c4], %0, %1, %2 offen offset:%c5 sc1" ::"v"(vo), "s"(srd), "s"(so), "i"(4 * N), "i"(4 * N + 3), "i"(IMM) : "memory"); }
template <int N, bool Z> DI void mfma32a(v16f& acc, v4u b) {
  if constexpr (Z) asm volatile("v_mfma_f32_32x32x16_bf16 %0, a[%c2:%c3], %1, 0" : "=&v"(acc) : "v"(b), "i"(4 * N), "i"(4 * N + 3));
  else asm volatile("v_mfma_f32_32x32x16_bf16 %0, a[%c2:%c3], %1, %0" : "+v"(acc) : "v"(b), "i"(4 * N), "i"(4 * N + 3));
}
template <bool Z> DI void mfma16(v4f& acc, v4u a, v4u b) {
  if constexpr (Z) asm volatile("v_mfma_f32_16x16x32_bf16 %0, %1, %2, 0" : "=&v"(acc) : "v"(a), "v"(b));
  else asm volatile("v_mfma_f32_16x16x32_bf16 %0, %1, %2, %0" : "+v"(acc) : "v"(a), "v"(b));
}
template <int I, int N, class Fn> DI void sfor(Fn&& f) { if constexpr (I < N) { f(std::integral_constant<int, I>{}); sfor<I + 1, N>(f); } }
DI void poll(const char* p, u64 want) {
  for (u32 sp = 0; sp < SPIN_CAP; ++sp) { if (__all(__hip_atomic_load((GAS u64*)p, RLX_AGENT) == want)) break; }
  ORDER();
}
#define STAMP(n)
__device__ constexpr int CP[36] = {0, 0,1,1, 0,2,1,2,2, 0,3,1,3,2,3,3, 0,4,1,4,2,4,3,4, 0,5,1,5,2,5, 0,6,1,6, 0,7};
__device__ constexpr int CQ[36] = {0, 1,0,1, 2,0,2,1,2, 3,0,3,1,3,2,3, 4,0,4,1,4,2,4,3, 5,0,5,1,5,2, 6,0,6,1, 7,0};
constexpr bool first_of(int m) { for (int x = 0; x < m; ++x) if (CP[x] + CQ[x] == CP[m] + CQ[m]) return false; return true; }

template <int MASK>
__global__ __launch_bounds__(256, 1) void mega(Params P) {
  constexpr bool FUSED = MASK == 31;
  extern __shared__ __attribute__((aligned(16))) char smem_[];
  asm volatile("" ::: "a0", "a255");
  const int b = blockIdx.x, t = threadIdx.x, lane = t & 63, w = __builtin_amdgcn_readfirstlane(t >> 6);
  const int j = b & 7, i = b >> 3;
  const rsrc_t rws = mkrs(P.ws, WS_BYTES);
  const v4u sws = mksrd(P.ws, WS_BYTES);
  const int g = lane >> 4, i16 = lane & 15, q4 = i16 >> 2, p4 = lane & 3;
  const bool is_stat = i >= 28;
  const int mt = 4 * (j >> 1) + (i & 3), nt = 7 * (j & 1) + (i >> 2);
  const int rb = 4 * j + (i - 28), tvt = rb * 4 + w;
  const int mt2 = 4 * j + (i & 3), dt = i >> 2;
  STAMP(0);
  u64 tag = 0, ep = 0;
  if constexpr (FUSED) ep = __hip_atomic_load((GAS u64*)(P.ws + EPOCH_OFF), RLX_AGENT);
  v4u sk0, sk1, sk2, sk3;
  auto load_sk = [&]() {
    const rsrc_t r = mkrs(P.sk, D * S * 2);
    const int tt = t & 127; const u32 o = (64 * j + 2 * i + (t >> 7)) * 4096 + tt * 16;
    sk0 = ld16(r, tt ? o - 16 : 0x80000000u); sk1 = ld16(r, o); sk2 = ld16(r, o + 2032); sk3 = ld16(r, o + 2048);
  };
  auto build_kc = [&]() {
    const u32 base = KC_L + (t >> 7) * KC_CH + (1 + (t & 127)) * 16;
    auto one = [&](v4u a, v4u c, u32 ad) {
      u32 d[8] = {a.x, a.y, a.z, a.w, c.x, c.y, c.z, c.w}, s[7];
#pragma unroll
      for (int m = 0; m < 7; ++m) s[m] = __builtin_amdgcn_alignbit(d[m + 1], d[m], 16);
#pragma unroll
      for (int r = 0; r < 8; ++r) { v4u o; if (r & 1) { const int m = (r + 1) / 2; o = v4u{d[m], d[m + 1], d[m + 2], d[m + 3]}; } else { const int m = r / 2; o = v4u{s[m], s[m + 1], s[m + 2], s[m + 3]}; }
        sts16(ad + r * KC_CS, o); }
    };
    one(sk0, sk1, base); one(sk2, sk3, base + 2048);
    const v4u z = {0, 0, 0, 0};
    if (t < 16) sts16(KC_L + (t >> 3) * KC_CH + (t & 7) * KC_CS, z);
    if (t >= 64 && t < 128 && (t & 31) < 30) sts16(WL_L + ((t >> 5) & 1) * WL_CH + 4096 + (t & 31) * 16, z);
  };

  if constexpr (MASK & 1) {
    const int c8 = t & 7, rp = t >> 3;
    v4u kk[5], vv[2], sh[4];
    { const rsrc_t rk = mkrs(P.k, S * D * 2), rv = mkrs(P.v, S * D * 2), rs = mkrs(P.sh, D * 8);
      const int r0 = 64 * i + 2 * rp; const u32 col = (64 * j + 8 * c8) * 2;
#pragma unroll
      for (int m = 0; m < 5; ++m) kk[m] = ld16(rk, (u32)((r0 - 3 + m) * 1024) + col);
      vv[0] = ld16(rv, r0 * 1024 + col); vv[1] = ld16(rv, r0 * 1024 + col + 1024);
#pragma unroll
      for (int m = 0; m < 4; ++m) sh[m] = ld16(rs, col * 4 + 16 * m); }
    if constexpr (FUSED) load_sk();
    v4u side[16]; u32 cr[2];
    const bool do_mod = b < 192, do_wd = b < 224, do_w = b >= 32;
    const int cg = b % 48, ksp = b / 48, fb = b >> 3, wj = b - 32;
    if (w == 1) {
      const rsrc_t rq = mkrs(P.q, S * D * 2); const v4u sq = mksrd(P.q, S * D * 2);
#pragma unroll
      for (int r = 0; r < 8; ++r) side[r] = ld16(rq, (8 * b + r) * 1024 + lane * 16);
#pragma unroll
      for (int n = 0; n < 8; ++n) { const int f = ((lane >> 4) & 1) | ((n & 1) << 1), cs = ((((lane & 7) >> 1) ^ f) << 1) | (lane & 1);
        glds(sq, (lane >> 3) * 1024 + cs * 16, (64 * i + 8 * n) * 1024 + 128 * j, QL_L + n * 1024); }
    } else if (w == 2) {
      if (do_mod) { const rsrc_t rm = mkrs(P.wm, D * 6 * D * 2), rc = mkrs(P.c, D * 2);
        cr[0] = __builtin_amdgcn_raw_buffer_load_b16(rc, (128 * ksp + lane) * 2, 0, 0); cr[1] = __builtin_amdgcn_raw_buffer_load_b16(rc, (128 * ksp + 64 + lane) * 2, 0, 0);
#pragma unroll
        for (int n = 0; n < 16; ++n) side[n] = ld16(rm, (lane >> 3) * 6144 + (lane & 7) * 16, (128 * ksp + 8 * n) * 6144 + 128 * cg); }
      if (do_wd) { const v4u sd = mksrd(P.wd, F * D * 2); const int cs = (lane & 7) ^ (((lane >> 4) & 1) << 2);
#pragma unroll
        for (int n = 0; n < 8; ++n) glds(sd, (lane >> 3) * 1024 + cs * 16, (64 * fb + 8 * n) * 1024 + 128 * j, WDS_L + n * 1024); }
    } else if (w == 3) {
      if (do_w) { const v4u sg = mksrd(P.wg, D * F * 2), su = mksrd(P.wu, D * F * 2); const int sl = lane & 15, wn = wj >> 4, kb = wj & 15;
        const u32 vg = (lane >> 4) * 3584 + ((((sl >> 1) ^ (lane >> 4)) << 1) | (sl & 1)) * 16, vu = vg ^ 128;
#pragma unroll
        for (int n = 0; n < 8; ++n) { glds(sg, vg, (32 * kb + 4 * n) * 3584 + 256 * wn, WS_L + n * 1024); glds(su, vu, (32 * kb + 4 * n) * 3584 + 256 * wn, WS_L + 8192 + n * 1024); } }
    }
    SB();
    if constexpr (FUSED) tag = (((u64)(u32)__builtin_amdgcn_readfirstlane((u32)(ep >> 32)) << 32) | (u32)__builtin_amdgcn_readfirstlane((u32)ep)) + 1;
    if (t == 0) *(LAS u32*)MISC_L = 0;
    if constexpr (FUSED) if (w == 0 && i < 2 && lane < 16)
      __hip_atomic_store((GAS u64*)(P.ws + (lane < 8 ? CNT0_OFF : CNTB_OFF) + ((lane & 7) * 16 + 2 * j + i) * 128), tag << 8, RLX_AGENT);
    SB();
    { v4u o0, o1;
#pragma unroll
      for (int e = 0; e < 4; ++e) {
        const v2f t0 = {lo16(sh[e].x), lo16(sh[e].z)}, t1 = {hi16(sh[e].x), hi16(sh[e].z)}, t2 = {lo16(sh[e].y), lo16(sh[e].w)}, t3 = {hi16(sh[e].y), hi16(sh[e].w)};
        const v2f k0 = unpk(kk[0][e]), k1 = unpk(kk[1][e]), k2 = unpk(kk[2][e]), k3 = unpk(kk[3][e]), k4 = unpk(kk[4][e]);
        o0[e] = pk((k0 * t3 + k1 * t2 + k2 * t1 + k3 * t0) * unpk(vv[0][e]));
        o1[e] = pk((k1 * t3 + k2 * t2 + k3 * t1 + k4 * t0) * unpk(vv[1][e]));
      }
      const int f = (rp & 1) | (((rp >> 2) & 1) << 1);
      const u32 ad = KV_L + rp * 256 + (((c8 >> 1) ^ f) << 5) + ((c8 & 1) << 4);
      sts16(ad, o0); sts16(ad + 128, o1); }
    STAMP(1);
    BAR();
    auto transpose64 = [&](u32 tile, bool rev, u32 dst, u32 pos0) {
#pragma unroll
      for (int cb = 0; cb < 4; ++cb)
#pragma unroll
        for (int it = 0; it < 2; ++it) { const int m = 4 * it + g; v4s h[2];
#pragma unroll
          for (int tt = 0; tt < 2; ++tt) { const int x = 8 * m + 4 * tt + q4, r = rev ? 63 - x : x, f = ((r >> 1) & 1) | (((r >> 3) & 1) << 1);
            h[tt] = trr(tile + r * 128 + ((cb ^ f) << 5) + p4 * 8); }
          st16c(cat(h[0], h[1]), rws, (16 * cb + i16) * 4096 + m * 16, dst + 64 * j * 4096 + pos0 * 2); }
    };
    if (w == 0) {
      transpose64(KV_L, true, WT_OFF, 1984 - 64 * i);
      if constexpr (FUSED) { DRAIN(); if (lane == 0) __hip_atomic_store((GAS u64*)(P.ws + FLAGA_OFF + (j * 32 + i) * 128), tag, RLX_AGENT); }
      STAMP(2);
      if constexpr (FUSED) build_kc();
    } else {
      if constexpr (FUSED) build_kc();
      STAMP(2);
      if (w == 1) {
        v4f so[4];
#pragma unroll
        for (int r = 0; r < 8; ++r) { v2f a = unpk(side[r].x), c = unpk(side[r].y), d = unpk(side[r].z), e = unpk(side[r].w);
          const v2f s2 = (a + c) + (d + e); const float m = wsum64(s2.x + s2.y) * (1.f / D); const v2f mm = {m, m};
          a -= mm; c -= mm; d -= mm; e -= mm; const v2f q2 = a * a + c * c + d * d + e * e;
          so[r >> 1][(r & 1) * 2] = m; so[r >> 1][(r & 1) * 2 + 1] = rsqrtf(wsum64(q2.x + q2.y) * (1.f / D) + EPS); }
        if (lane == 0) {
#pragma unroll
          for (int r = 0; r < 4; ++r) st16c(__builtin_bit_cast(v4u, so[r]), rws, r * 16, ST1_OFF + b * 64); }
        DRAIN();
        transpose64(QL_L, false, QT_OFF, 64 * i);
      } else if (w == 2) {
        if (do_mod) { float sv[2];
#pragma unroll
          for (int n = 0; n < 2; ++n) { const float x = lo16(cr[n]); sv[n] = rbf(x / (1.f + __expf(-x))); }
          v2f a[4] = {};
#pragma unroll
          for (int n = 0; n < 16; ++n) { const float s = __builtin_bit_cast(float, __builtin_amdgcn_ds_bpermute((8 * (n & 7) + (lane >> 3)) * 4, __builtin_bit_cast(int, sv[n >> 3]))); const v2f ss = {s, s};
#pragma unroll
            for (int e = 0; e < 4; ++e) a[e] += ss * unpk(side[n][e]); }
          v4f o0 = {wsum_hi3(a[0].x), wsum_hi3(a[0].y), wsum_hi3(a[1].x), wsum_hi3(a[1].y)}, o1 = {wsum_hi3(a[2].x), wsum_hi3(a[2].y), wsum_hi3(a[3].x), wsum_hi3(a[3].y)};
          if (lane < 8) { st16c(__builtin_bit_cast(v4u, o0), rws, lane * 32, MODP_OFF + (ksp * 3072 + 64 * cg) * 4); st16c(__builtin_bit_cast(v4u, o1), rws, lane * 32 + 16, MODP_OFF + (ksp * 3072 + 64 * cg) * 4); } }
        if (do_wd) { DRAIN(); const int h = lane >> 5, blk = g & 1;
#pragma unroll
          for (int ks = 0; ks < 4; ++ks)
#pragma unroll
            for (int dl = 0; dl < 2; ++dl) { v4s x[2];
#pragma unroll
              for (int tt = 0; tt < 2; ++tt) x[tt] = trr(WDS_L + (16 * ks + 8 * tt + 4 * h + q4) * 128 + ((dl ^ (q4 >> 1)) << 6) + blk * 32 + p4 * 8);
              st16c(cat(x[0], x[1]), rws, lane * 16, WD_OFF + ((j * 112 + 4 * fb + ks) * 2 + dl) * 1024); } }
      } else {
        if (do_w) { DRAIN(); const int mat = g & 1, h = g >> 1, wn = wj >> 4, kb = wj & 15;
#pragma unroll
          for (int ft = 0; ft < 8; ++ft)
#pragma unroll
            for (int ks = 0; ks < 2; ++ks) { v4s x[2];
#pragma unroll
              for (int tt = 0; tt < 2; ++tt) x[tt] = trr(WS_L + mat * 8192 + (16 * ks + 8 * h + 4 * tt + q4) * 256 + ((ft ^ (q4 | (mat << 2))) << 5) + p4 * 8);
              st16c(cat(x[0], x[1]), rws, lane * 16, WG_OFF + (((wn * 4 + (ft >> 1)) * 32 + 2 * kb + ks) * 2 + (ft & 1)) * 1024); } }
      }
      if constexpr (FUSED) {
        const char* ini = P.ws + FLAGA_OFF + (j * 32 + (i & 1)) * 128;
        const u64 f0 = __hip_atomic_load((GAS u64*)ini, RLX_AGENT);
        DRAIN();
        u32 old = 0; if (lane == 0) old = __hip_atomic_fetch_add((LAS u32*)MISC_L, 1u, __ATOMIC_RELAXED, __HIP_MEMORY_SCOPE_WORKGROUP);
        if (__builtin_amdgcn_readfirstlane(old) == 2) { if (!__all(f0 == tag)) poll(ini, tag);
          if (lane < 8) __hip_atomic_fetch_add((GAS u64*)(P.ws + CNT0_OFF + (lane * 16 + 2 * j + (i & 1)) * 128), 1ull, RLX_AGENT); } }
      STAMP(3);
    }
  }

  if constexpr (MASK & 2) {
    if constexpr (!FUSED) { load_sk(); build_kc(); }
    if (w == 0) {
      if constexpr (FUSED) poll(P.ws + FLAGA_OFF + (j * 32 + (lane & 31)) * 128, tag);
      STAMP(3);
#pragma unroll
      for (int n = 0; n < 8; ++n) gldsc(sws, lane * 16, WT_OFF + (64 * j + 2 * i + (n >> 2)) * 4096 + (n & 3) * 1024, WL_L + (n >> 2) * WL_CH + (n & 3) * 1024);
      DRAIN();
    }
    STAMP(4);
    BAR();
    STAMP(5);
    const int ch = w >> 1, kh = w & 1, d = 64 * j + 2 * i + ch;
    const u32 aA = KC_L + ch * KC_CH + (i16 & 7) * KC_CS + ((i16 >> 3) + g) * 16 + kh * 256;
    const u32 aB = WL_L + ch * WL_CH + 480 - 32 * i16 + 16 * g + kh * 256;
    v4u fa[2][8], fb_[2][8]; v4f acc[8];
    v4u qv[2], st[2][4]; u32 mraw = 0, draw = 0;
#pragma unroll
    for (int m = 0; m < 8; ++m) { fa[0][m] = lds16(aA + 512 * m); fb_[0][m] = lds16(aB + 512 * (7 - m)); }
    SB();
    sfor<0, 4>([&](auto sc) { constexpr int s = sc;
      if constexpr (s == 2) {
        if constexpr (FUSED) poll(P.ws + CNT0_OFF + (j * 16 + i16) * 128, (tag << 8) + 16);
        STAMP(6);
#pragma unroll
        for (int pr = 0; pr < 2; ++pr) { const u32 s0 = 256 * (4 * kh + 2 * pr + (g & 1)) + 16 * i16 + 8 * (g >> 1);
          qv[pr] = ld16c(rws, s0 * 2, QT_OFF + d * 4096);
#pragma unroll
          for (int m = 0; m < 4; ++m) st[pr][m] = ld16c(rws, s0 * 8 + 16 * m, ST1_OFF); }
        mraw = ld4c(rws, (p4 * 3072 + (q4 == 3 ? 4 : q4) * 512) * 4, MODP_OFF + d * 4);
        draw = __builtin_amdgcn_raw_buffer_load_b16(mkrs(P.Dv, D * 2), 0, d * 2, 0);
        SB();
      }
      sfor<0, 36>([&](auto mc) { constexpr int m = mc;
        mfma16<s == 0 && first_of(m)>(acc[CP[m] + CQ[m]], fa[s & 1][CP[m]], fb_[s & 1][CQ[m]]);
        if constexpr (s < 3 && (m & 1) == 0 && m < 32) { constexpr int x = m >> 2;
          if constexpr (m & 2) fb_[(s + 1) & 1][x] = lds16(aB + 512 * (7 - x) + 64 * (s + 1)); else fa[(s + 1) & 1][x] = lds16(aA + 512 * x + 64 * (s + 1)); }
        SB(); });
    });
    asm volatile("s_nop 7\n\ts_nop 1" : "+v"(acc[0]), "+v"(acc[1]), "+v"(acc[2]), "+v"(acc[3]), "+v"(acc[4]), "+v"(acc[5]), "+v"(acc[6]), "+v"(acc[7])::"memory");
    STAMP(7);
    v4f y[4];
#pragma unroll
    for (int m = 0; m < 4; ++m) { sts16(RED_L + w * 4096 + m * 1024 + lane * 16, __builtin_bit_cast(v4u, kh ? acc[m] : acc[4 + m])); y[m] = kh ? acc[4 + m] : acc[m]; }
    BAR();
#pragma unroll
    for (int m = 0; m < 4; ++m) y[m] += __builtin_bit_cast(v4f, lds16(RED_L + (w ^ 1) * 4096 + m * 1024 + lane * 16));
    const float mq = rbf(dppadd<0x4E>(dppadd<0xB1>(__builtin_bit_cast(float, mraw))));
    auto rl = [&](int l) { return __builtin_bit_cast(float, __builtin_amdgcn_readlane(__builtin_bit_cast(int, mq), l)); };
    const float sa = rl(0), a1 = rbf(1.f + rl(4)), ga = rl(8), sg = rbf(1.f + rl(12)), dv = lo16(draw);
#pragma unroll
    for (int pr = 0; pr < 2; ++pr) { const u32 s0 = 256 * (4 * kh + 2 * pr + (g & 1)) + 16 * i16 + 8 * (g >> 1);
      float yy[8];
#pragma unroll
      for (int e = 0; e < 4; ++e) { const float ya = y[2 * pr][e], yb = y[2 * pr + 1][e]; auto r = __builtin_amdgcn_permlane16_swap(__builtin_bit_cast(u32, ya), __builtin_bit_cast(u32, yb), false, false);
        yy[e] = __builtin_bit_cast(float, (u32)r[0]); yy[4 + e] = __builtin_bit_cast(float, (u32)r[1]); }
      const v4u uw = lds16(WL_L + ch * WL_CH + (2040 - s0) * 2);
      v4u o1, o2;
#pragma unroll
      for (int e = 0; e < 4; ++e) { const v2f x = unpk(qv[pr][e]), u = {hi16(uw[3 - e]), lo16(uw[3 - e])}, yv = {yy[2 * e], yy[2 * e + 1]};
        const v4f sm = __builtin_bit_cast(v4f, st[pr][e]); const v2f mn = {sm.x, sm.z}, rs = {sm.y, sm.w};
        const v2f h = (x - mn) * rs * a1 + sa;
        o1[e] = pk(x + ga * (h * (yv + u * dv)));
        o2[e] = pk(unpk(o1[e]) * sg); }
      st16c(o1, rws, s0 * 2, X1_OFF + d * 4096); st16c(o2, rws, s0 * 2, X1S_OFF + d * 4096); }
    STAMP(8);
    if constexpr (FUSED) { DRAIN(); BAR();
      if (w == 0 && lane < 8) __hip_atomic_fetch_add((GAS u64*)(P.ws + CNTB_OFF + (lane * 16 + 2 * j + (i & 1)) * 128), 1ull, RLX_AGENT); }
    STAMP(9);
  }

  const u32 wdso = WD_OFF + ((dt * 112 + 28 * w) * 2) * 1024;
  auto res_tile = [&]() {
    const int cs = (lane & 7) ^ (((lane >> 4) & 1) << 2);
#pragma unroll
    for (int n = 0; n < 2; ++n) gldsc(sws, (lane >> 3) * 4096 + cs * 16, X1_OFF + (64 * dt + 16 * w + 8 * n) * 4096 + 128 * mt2, X1L_L + (2 * w + n) * 1024);
  };
  if constexpr (MASK & 4) if (is_stat) {
    if (tvt < 112) sfor<0, 32>([&](auto c) { constexpr int ks = c; lda<ks, (ks & 1) * 2048>(sws, lane * 16, WG_OFF + (tvt >> 1) * 65536 + (tvt & 1) * 1024 + (ks >> 1) * 4096); });
    { v2f cb = {0, 0}, sb = {0, 0};
#pragma unroll
      for (int m = 0; m < 4; ++m) { cb += __builtin_bit_cast(v2f, ld8c(rws, t * 8, MODP_OFF + (m * 3072 + 4 * 512) * 4)); sb += __builtin_bit_cast(v2f, ld8c(rws, t * 8, MODP_OFF + (m * 3072 + 3 * 512) * 4)); }
      *(LAS u32*)(SIG_L + t * 4) = pk(unpk(pk(cb)) + 1.f); *(LAS u32*)(SIG_L + 1024 + t * 4) = pk(sb); }
    DRAIN(); BAR();
    if (tvt < 112) { v16f a; const u32 ad = SIG_L + (lane & 1) * 1024 + (lane >> 5) * 16;
      sfor<0, 32>([&](auto c) { constexpr int ks = c; mfma32a<ks, ks == 0>(a, lds16(ad + 32 * ks)); });
      asm volatile("s_nop 7\n\ts_nop 7" : "+v"(a)::"memory");
      if ((lane & 31) < 2) {
#pragma unroll
        for (int m = 0; m < 4; ++m) st16c(__builtin_bit_cast(v4u, v4f{a[4 * m], a[4 * m + 1], a[4 * m + 2], a[4 * m + 3]}), rws, (lane >> 5) * 128 + (lane & 1) * 64 + m * 16, TV_OFF + tvt * 256); } }
    if constexpr (FUSED) { if (w == 0) poll(P.ws + CNTB_OFF + (j * 16 + i16) * 128, (tag << 8) + 16); BAR(); }
    STAMP(10);
    v4u x[16];
#pragma unroll
    for (int n = 0; n < 16; ++n) x[n] = ld16c(rws, (t >> 3) * 4096 + (t & 7) * 16, X1_OFF + n * 32 * 4096 + 128 * rb);
    v2f a[4] = {};
#pragma unroll
    for (int n = 0; n < 16; ++n)
#pragma unroll
      for (int e = 0; e < 4; ++e) a[e] += unpk(x[n][e]);
    auto xwave = [&](v2f (&v)[4], u32 buf) {
      const v4f o0 = {wsum_hi3(v[0].x), wsum_hi3(v[0].y), wsum_hi3(v[1].x), wsum_hi3(v[1].y)}, o1 = {wsum_hi3(v[2].x), wsum_hi3(v[2].y), wsum_hi3(v[3].x), wsum_hi3(v[3].y)};
      if (lane < 8) { sts16(buf + w * 256 + lane * 32, __builtin_bit_cast(v4u, o0)); sts16(buf + w * 256 + lane * 32 + 16, __builtin_bit_cast(v4u, o1)); }
      BAR();
      v4f r0 = {}, r1 = {};
#pragma unroll
      for (int u = 0; u < 4; ++u) { r0 += __builtin_bit_cast(v4f, lds16(buf + u * 256 + (t & 7) * 32)); r1 += __builtin_bit_cast(v4f, lds16(buf + u * 256 + (t & 7) * 32 + 16)); }
      v[0] = v2f{r0.x, r0.y} * (1.f / D); v[1] = v2f{r0.z, r0.w} * (1.f / D); v[2] = v2f{r1.x, r1.y} * (1.f / D); v[3] = v2f{r1.z, r1.w} * (1.f / D);
    };
    xwave(a, RED_L);
    v2f vq[4] = {};
#pragma unroll
    for (int n = 0; n < 16; ++n)
#pragma unroll
      for (int e = 0; e < 4; ++e) { const v2f dd = unpk(x[n][e]) - a[e]; vq[e] += dd * dd; }
    xwave(vq, RED_L + 1024);
    if (t < 8) {
#pragma unroll
      for (int e = 0; e < 4; ++e) st16c(__builtin_bit_cast(v4u, v4f{a[e].x, rsqrtf(vq[e].x + EPS), a[e].y, rsqrtf(vq[e].y + EPS)}), rws, t * 64 + e * 16, ST2_OFF + rb * 512); }
    if constexpr (FUSED) { DRAIN(); BAR(); if (t == 0) __hip_atomic_store((GAS u64*)(P.ws + FLAGS_OFF + rb * 128), tag, RLX_AGENT);
      res_tile();
      sfor<0, 56>([&](auto c) { constexpr int n = c; lda<n, (n & 3) * 1024>(sws, lane * 16, wdso + (n >> 2) * 4096); }); }
    STAMP(13);
  }

  if constexpr (MASK & 8) if (!is_stat) {
    const u32 wso = WG_OFF + (nt * 4 + w) * 65536;
    if (w != 0) asm volatile("s_barrier" ::: "memory");
    sfor<0, 32>([&](auto c) { constexpr int n = c; lda<n, (n & 3) * 1024>(sws, lane * 16, wso + (n >> 2) * 4096); });
    if (w == 0) { asm volatile("s_barrier" ::: "memory");
      if constexpr (FUSED) { poll(P.ws + CNTB_OFF + (j * 16 + i16) * 128, (tag << 8) + 16);
        if (b == 0 && lane == 0) __hip_atomic_store((GAS u64*)(P.ws + EPOCH_OFF), tag, RLX_AGENT); } }
    asm volatile("s_barrier" ::: "memory");
    STAMP(10);
    const u32 xo = (4 * w + g) * 4096 + ((i16 ^ ((g << 2) | w)) << 4);
    auto stageA = [&](int ks) { gldsc(sws, xo, X1S_OFF + ks * 65536 + mt * 256, A_L + ks * 4096 + w * 1024); };
#pragma unroll
    for (int ks = 0; ks < 8; ++ks) stageA(ks);
    u32 base[4][2];
#pragma unroll
    for (int c = 0; c < 4; ++c)
#pragma unroll
      for (int tt = 0; tt < 2; ++tt) { const u32 row = 8 * (lane >> 5) + 4 * tt + q4, chk = 4 * c + 2 * (g & 1) + (p4 >> 1);
        base[c][tt] = A_L + 256u * row + 16u * (chk ^ (((row & 3) << 2) | ((row >> 2) & 3))) + 8 * (p4 & 1); }
    v16f acc[2][4]; v4s f[2][4][2]; u64 fl = 0;
    asm volatile("s_waitcnt vmcnt(5)\n\ts_barrier" ::: "memory");
    STAMP(11);
#pragma unroll
    for (int c = 0; c < 4; ++c) { f[0][c][0] = trr(base[c][0]); f[0][c][1] = trr(base[c][1]); }
    const char* fsp = P.ws + FLAGS_OFF + (lane & 31) * 128;
    sfor<0, 32>([&](auto kc) { constexpr int ks = kc;
      if constexpr (ks == 2) asm volatile("s_waitcnt vmcnt(6)\n\ts_barrier" ::: "memory");
      if constexpr (ks == 7) asm volatile("s_waitcnt vmcnt(9)\n\ts_barrier" ::: "memory");
      if constexpr (ks == 19) asm volatile("s_waitcnt vmcnt(22)\n\ts_barrier" ::: "memory");
      else if constexpr (ks >= 16) WAITV(22);
      if constexpr (ks == 15) {
#pragma unroll
        for (int c = 0; c < 4; ++c) { base[c][0] += 65536; base[c][1] += 65536; asm volatile("" : "+v"(base[c][0]), "+v"(base[c][1])); } }
      if constexpr (FUSED && ks == 22) asm volatile("global_load_dwordx2 %0, %1, off sc1" : "=v"(fl) : "v"(fsp) : "memory");
      if constexpr (ks == 26) {
        if constexpr (FUSED) { u32 n = SPIN_CAP;
          asm volatile("s_waitcnt vmcnt(12)\n"
                       "1:\n\tv_cmp_ne_u64 vcc, %3, %0\n\ts_nop 4\n\ts_cbranch_vccz 2f\n\ts_sub_u32 %1, %1, 1\n\ts_cbranch_scc1 2f\n\t"
                       "global_load_dwordx2 %0, %2, off sc1\n\ts_waitcnt vmcnt(0)\n\ts_branch 1b\n2:"
                       : "+v"(fl), "+s"(n) : "v"(fsp), "s"(tag) : "vcc", "scc", "memory"); }
        gldsc(sws, lane * 16, ST2_OFF + mt * 1024, EPI_L + w * 2048); gldsc(sws, lane * 16, TV_OFF + (nt * 8 + 2 * w) * 256, EPI_L + w * 2048 + 1024); }
      SB();
      sfor<0, 8>([&](auto mc) { constexpr int c = mc >> 1, T = mc & 1;
        mfma32a<2 * ks + T, ks == 0>(acc[T][c], cat(f[ks & 1][c][0], f[ks & 1][c][1]));
        if constexpr (ks < 31) f[(ks + 1) & 1][c][T] = trr(base[c][T] + ((ks + 1) * 4096 - (ks >= 15 ? 65536 : 0)));
        if constexpr (T == 1 && c < 3) { constexpr int z = 3 * ks + c;
          if constexpr (z < 24) stageA(8 + z);
          else if constexpr (z < 56) lda<z + 8, (z & 3) * 1024>(sws, lane * 16, wso + ((z + 8) >> 2) * 4096);
          else if constexpr (FUSED) lda<z - 56, (z & 3) * 1024>(sws, lane * 16, wdso + ((z - 56) >> 2) * 4096);
          else asm volatile("buffer_load_dword a0, %0, %1, 0 offen" ::"v"(lane), "s"(sws) : "memory"); }
        SB(); });
    });
    asm volatile("s_waitcnt vmcnt(18)\n\ts_nop 7\n\ts_nop 7" : "+v"(acc[0][0]), "+v"(acc[0][1]), "+v"(acc[0][2]), "+v"(acc[0][3]), "+v"(acc[1][0]), "+v"(acc[1][1]), "+v"(acc[1][2]), "+v"(acc[1][3])::"memory");
    STAMP(12);
    v2f mr[4];
#pragma unroll
    for (int c = 0; c < 4; ++c) { const v2f s2 = *(LAS v2f*)(EPI_L + w * 2048 + (32 * c + (lane & 31)) * 8); mr[c] = v2f{s2.y, -s2.x * s2.y}; }
#pragma unroll
    for (int T = 0; T < 2; ++T) { v4f t1[4], t2[4];
#pragma unroll
      for (int m = 0; m < 4; ++m) { const u32 ad = EPI_L + w * 2048 + 1024 + T * 256 + (lane >> 5) * 128 + m * 16; t1[m] = __builtin_bit_cast(v4f, lds16(ad)); t2[m] = __builtin_bit_cast(v4f, lds16(ad + 64)); }
#pragma unroll
      for (int c = 0; c < 4; ++c) { v4u o;
#pragma unroll
        for (int e = 0; e < 4; ++e) { const int r = 2 * e;
          const v2f ag = {acc[T][c][r], acc[T][c][r + 1]}, au = {acc[T][c][8 + r], acc[T][c][9 + r]};
          const v2f g1 = {t1[r >> 2][r & 3], t1[r >> 2][(r & 3) + 1]}, g2 = {t2[r >> 2][r & 3], t2[r >> 2][(r & 3) + 1]};
          const v2f u1 = {t1[2 + (r >> 2)][r & 3], t1[2 + (r >> 2)][(r & 3) + 1]}, u2 = {t2[2 + (r >> 2)][r & 3], t2[2 + (r >> 2)][(r & 3) + 1]};
          o[e] = pk((ag * mr[c].x + (g1 * mr[c].y + g2)) * (au * mr[c].x + (u1 * mr[c].y + u2))); }
        st16c(o, rws, lane * 16, HH_OFF + ((4 * mt + c) * 112 + 8 * nt + 2 * w + T) * 1024); } }
    STAMP(13);
    if constexpr (FUSED) { DRAIN(); if (lane == 0) __hip_atomic_store((GAS u64*)(P.ws + FLAGC_OFF + (mt * 56 + nt * 4 + w) * 128), tag, RLX_AGENT);
      res_tile();
      sfor<40, 56>([&](auto c) { constexpr int n = c; lda<n, (n & 3) * 1024>(sws, lane * 16, wdso + (n >> 2) * 4096); }); }
  }

  if constexpr (MASK & 16) {
    if constexpr (!FUSED) { res_tile(); sfor<0, 56>([&](auto c) { constexpr int n = c; lda<n, (n & 3) * 1024>(sws, lane * 16, wdso + (n >> 2) * 4096); }); }
    { const float gq = dppadd<0x4E>(dppadd<0xB1>(__builtin_bit_cast(float, ld4c(rws, (p4 * 3072 + (t >> 2)) * 4, MODP_OFF + (5 * 512 + 64 * dt) * 4))));
      if (p4 == 0) *(LAS float*)(GB_L + (t >> 2) * 4) = rbf(gq); }
    BAR();
    STAMP(14);
    if constexpr (FUSED) poll(P.ws + FLAGC_OFF + ((mt2 >> 1) * 56 + 14 * w + (i16 < 14 ? i16 : 13)) * 128, tag);
    DRAIN();
    STAMP(15);
    constexpr int WIN = 18;
    v4u hf[WIN][2]; v16f acc[2][2];
    const u32 hso = HH_OFF + (2 * mt2 * 112 + 28 * w) * 1024;
#pragma unroll
    for (int l = 0; l < WIN; ++l) { hf[l][0] = ld16c(rws, lane * 16 + (l & 3) * 1024, hso + (l >> 2) * 4096); hf[l][1] = ld16c(rws, lane * 16 + (l & 3) * 1024, hso + 112 * 1024 + (l >> 2) * 4096); }
    SB();
    sfor<0, 28>([&](auto lc) { constexpr int l = lc;
      mfma32a<2 * l, l == 0>(acc[0][0], hf[l % WIN][0]); mfma32a<2 * l + 1, l == 0>(acc[1][0], hf[l % WIN][0]);
      mfma32a<2 * l, l == 0>(acc[0][1], hf[l % WIN][1]); mfma32a<2 * l + 1, l == 0>(acc[1][1], hf[l % WIN][1]);
      if constexpr (l + WIN < 28) { constexpr int n = l + WIN;
        hf[l][0] = ld16c(rws, lane * 16 + (n & 3) * 1024, hso + (n >> 2) * 4096); hf[l][1] = ld16c(rws, lane * 16 + (n & 3) * 1024, hso + 112 * 1024 + (n >> 2) * 4096); }
      SB(); });
    asm volatile("s_nop 7\n\ts_nop 7" : "+v"(acc[0][0]), "+v"(acc[0][1]), "+v"(acc[1][0]), "+v"(acc[1][1])::"memory");
    STAMP(16);
#pragma unroll
    for (int o = 0; o < 4; ++o) if (o != w) {
#pragma unroll
      for (int m = 0; m < 4; ++m) { const v16f& a = acc[o >> 1][o & 1]; sts16((o * 4 + w) * 4096 + m * 1024 + lane * 16, __builtin_bit_cast(v4u, v4f{a[4 * m], a[4 * m + 1], a[4 * m + 2], a[4 * m + 3]})); } }
    v16f r = (w & 2) ? ((w & 1) ? acc[1][1] : acc[1][0]) : ((w & 1) ? acc[0][1] : acc[0][0]);
    BAR();
#pragma unroll
    for (int u = 1; u < 4; ++u)
#pragma unroll
      for (int m = 0; m < 4; ++m) { const v4f x = __builtin_bit_cast(v4f, lds16((w * 4 + (w ^ u)) * 4096 + m * 1024 + lane * 16));
        r[4 * m] += x.x; r[4 * m + 1] += x.y; r[4 * m + 2] += x.z; r[4 * m + 3] += x.w; }
    const int dl = w >> 1, sl = w & 1, h = lane >> 5;
    v2u o[4];
#pragma unroll
    for (int G = 0; G < 4; ++G) { const v2u xr = __builtin_bit_cast(v2u, trr(X1L_L + (32 * dl + 8 * G + 4 * h + q4) * 128 + ((sl ^ (q4 >> 1)) << 6) + (g & 1) * 32 + p4 * 8));
      const v4f gb = __builtin_bit_cast(v4f, lds16(GB_L + (32 * dl + 8 * G + 4 * h) * 4));
      o[G].x = pk(unpk(xr.x) + v2f{gb.x, gb.y} * v2f{r[4 * G], r[4 * G + 1]}); o[G].y = pk(unpk(xr.y) + v2f{gb.z, gb.w} * v2f{r[4 * G + 2], r[4 * G + 3]}); }
    const rsrc_t ro = mkrs(P.out, S * D * 2);
#pragma unroll
    for (int G = 0; G < 4; G += 2) { auto r0 = __builtin_amdgcn_permlane32_swap(o[G].x, o[G + 1].x, false, false); auto r1 = __builtin_amdgcn_permlane32_swap(o[G].y, o[G + 1].y, false, false);
      st16(v4u{(u32)r0[0], (u32)r1[0], (u32)r0[1], (u32)r1[1]}, ro, (32 * sl + (lane & 31)) * 1024 + (8 * G + 8 * h) * 2, 64 * mt2 * 1024 + (64 * dt + 32 * dl) * 2); }
    STAMP(17);
  }
}

template <int MASK> static void launch(const Params& p, hipStream_t st) {
  hipFuncSetAttribute((const void*)mega<MASK>, hipFuncAttributeMaxDynamicSharedMemorySize, LDS_BYTES);
  mega<MASK><<<256, 256, LDS_BYTES, st>>>(p);
}
extern "C" void kernel_launch(void* const* d_in, const int* in_sizes, int n_in, void* d_out, int out_size, void* d_ws, size_t ws_size, hipStream_t stream) {
  Params p;
  p.q = (const u16*)d_in[0]; p.k = (const u16*)d_in[1]; p.v = (const u16*)d_in[2]; p.sh = (const u16*)d_in[3]; p.sk = (const u16*)d_in[4]; p.Dv = (const u16*)d_in[5];
  p.wg = (const u16*)d_in[7]; p.wu = (const u16*)d_in[8]; p.wd = (const u16*)d_in[9]; p.c = (const u16*)d_in[10]; p.wm = (const u16*)d_in[11];
  p.out = (u16*)d_out; p.ws = (char*)d_ws;
  launch<31>(p, stream);
}
```

```cpp
#include <hip/hip_runtime.h>
typedef unsigned short u16; typedef unsigned u32; typedef unsigned long long u64;
typedef u32 v2u __attribute__((ext_vector_type(2)));
typedef u32 v4u __attribute__((ext_vector_type(4)));
typedef float v2f __attribute__((ext_vector_type(2)));
typedef float v4f __attribute__((ext_vector_type(4)));
typedef float v16f __attribute__((ext_vector_type(16)));
typedef short v4s __attribute__((ext_vector_type(4)));
typedef short v8s __attribute__((ext_vector_type(8)));
typedef __bf16 v2bf __attribute__((ext_vector_type(2)));
#define LAS __attribute__((address_space(3)))
#define GAS __attribute__((address_space(1)))
#define DI __device__ __forceinline__
#define SB() __builtin_amdgcn_sched_barrier(0)
#define RLX_AGENT __ATOMIC_RELAXED, __HIP_MEMORY_SCOPE_AGENT
#define FUSED_LAUNCH 1
#define PF_BYTES 46080
#define STAMPS 0

constexpr int S = 2048, D = 512, F = 1792;
constexpr float EPS = 1e-5f;
constexpr u32 EPOCH_OFF = 0, FLAGA_OFF = 0x1000, CNT0_OFF = 0xA000, CNTB_OFF = 0xE000, FLAGS_OFF = 0x12000, FLAGC_OFF = 0x14000;
constexpr u32 ST1_OFF = 0x40000, ST2_OFF = 0x44000, MODP_OFF = 0x48000, TV_OFF = 0x58000, DBG_OFF = 0x80000;
constexpr u32 WT_OFF = 1u << 20, QT_OFF = 3u << 20, X1_OFF = 5u << 20, X1S_OFF = 7u << 20, WG_OFF = 9u << 20, WD_OFF = 13u << 20, HH_OFF = 15u << 20, WS_BYTES = 22u << 20;
constexpr u32 KC_L = 0, KC_CH = 33280, KC_CS = 4160;
constexpr u32 WL_L = 66560, WL_CH = 4608;
constexpr u32 RED_L = 75776;
constexpr u32 KV_L = 92160, QL_L = 100352, WS_L = 108544, WDS_L = 124928;
constexpr u32 WM_L = 133120, SV_L = 149504;
constexpr u32 A_L = 0;
constexpr u32 EPI_L = 133120;
constexpr u32 SIG_L = 141312;
constexpr u32 X1L_L = 143360, GB_L = 151552;
constexpr u32 MISC_L = 151808, STMP_L = 152064, LDS_BYTES = 153600;
constexpr u32 SPIN_CAP = 1u << 17;

struct Params { const u16 *q, *k, *v, *sh, *sk, *Dv, *wg, *wu, *wd, *c, *wm; u16* out; char* ws; };

typedef __amdgpu_buffer_rsrc_t rsrc_t;
DI rsrc_t mkrs(const void* p, u32 bytes) { return __builtin_amdgcn_make_buffer_rsrc((void*)p, 0, bytes, 0x00020000); }
DI v4u mksrd(const void* p, u32 bytes) { v4u r = {(u32)(size_t)p, (u32)((size_t)p >> 32), bytes, 0x00020000u}; return r; }
DI v4u ld16(rsrc_t r, u32 vo, u32 so = 0) { return __builtin_amdgcn_raw_buffer_load_b128(r, vo, so, 0); }
DI v4u ld16c(rsrc_t r, u32 vo, u32 so = 0) { return __builtin_amdgcn_raw_buffer_load_b128(r, vo, so, 16); }
DI u32 ld4c(rsrc_t r, u32 vo, u32 so = 0) { return __builtin_amdgcn_raw_buffer_load_b32(r, vo, so, 16); }
DI v2u ld8c(rsrc_t r, u32 vo, u32 so = 0) { return __builtin_amdgcn_raw_buffer_load_b64(r, vo, so, 16); }
DI void st16c(v4u v, rsrc_t r, u32 vo, u32 so = 0) { __builtin_amdgcn_raw_buffer_store_b128(v, r, vo, so, 16); asm volatile("s_nop 1" ::"v"(v) : "memory"); }
DI void st16(v4u v, rsrc_t r, u32 vo, u32 so = 0) { __builtin_amdgcn_raw_buffer_store_b128(v, r, vo, so, 0); asm volatile("s_nop 1" ::"v"(v) : "memory"); }
DI float lo16(u32 x) { return __builtin_bit_cast(float, x << 16); }
DI float hi16(u32 x) { return __builtin_bit_cast(float, x & 0xffff0000u); }
DI v2f unpk(u32 x) { v2f r = {lo16(x), hi16(x)}; return r; }
DI u32 pk(v2f v) { return __builtin_bit_cast(u32, __builtin_convertvector(v, v2bf)); }
DI u32 pk(float a, float b) { v2f v = {a, b}; return pk(v); }
DI float rbf(float a) { return lo16(pk(a, a)); }
DI v4s trr(u32 a) { return __builtin_amdgcn_ds_read_tr16_b64_v4i16((LAS v4s*)a); }
DI v4u cat(v4s a, v4s b) { return __builtin_bit_cast(v4u, __builtin_shufflevector(a, b, 0, 1, 2, 3, 4, 5, 6, 7)); }
DI v4u lds16(u32 a) { return *(LAS v4u*)a; }
DI void sts16(u32 a, v4u v) { *(LAS v4u*)a = v; }
template <int C> DI float dppadd(float x) { return x + __builtin_bit_cast(float, __builtin_amdgcn_update_dpp(0, __builtin_bit_cast(int, x), C, 0xf, 0xf, true)); }
DI float swap16add(float x) { u32 u = __builtin_bit_cast(u32, x); auto r = __builtin_amdgcn_permlane16_swap(u, u, false, false); return __builtin_bit_cast(float, (u32)r[0]) + __builtin_bit_cast(float, (u32)r[1]); }
DI float swap32add(float x) { u32 u = __builtin_bit_cast(u32, x); auto r = __builtin_amdgcn_permlane32_swap(u, u, false, false); return __builtin_bit_cast(float, (u32)r[0]) + __builtin_bit_cast(float, (u32)r[1]); }
DI float wsum64(float x) { x = dppadd<0xB1>(x); x = dppadd<0x4E>(x); x = dppadd<0x141>(x); x = dppadd<0x140>(x); x = swap16add(x); return swap32add(x); }
DI float wsum_hi3(float x) { x = dppadd<0x128>(x); x = swap16add(x); return swap32add(x); }
#define DRAIN() asm volatile("s_waitcnt vmcnt(0)" ::: "memory")
#define WAITV(n) asm volatile("s_waitcnt vmcnt(%0)" ::"i"(n) : "memory")
#define BAR() do { SB(); asm volatile("s_waitcnt lgkmcnt(0)\n\ts_barrier" ::: "memory"); SB(); } while (0)
#define ORDER() __builtin_amdgcn_fence(__ATOMIC_ACQUIRE, "wavefront")
DI void glds(v4u srd, u32 vo, u32 so, u32 ldst) { asm volatile("s_mov_b32 m0, %0\n\ts_nop 0\n\tbuffer_load_dwordx4 %1, %2, %3 offen lds" ::"s"(ldst), "v"(vo), "s"(srd), "s"(so) : "memory"); }
DI void gldsc(v4u srd, u32 vo, u32 so, u32 ldst) { asm volatile("s_mov_b32 m0, %0\n\ts_nop 0\n\tbuffer_load_dwordx4 %1, %2, %3 offen sc1 lds" ::"s"(ldst), "v"(vo), "s"(srd), "s"(so) : "memory"); }
template <int N, int IMM> DI void lda(v4u srd, u32 vo, u32 so) { asm volatile("buffer_load_dwordx4 a[%c3:%c4], %0, %1, %2 offen offset:%c5 sc1" ::"v"(vo), "s"(srd), "s"(so), "i"(4 * N), "i"(4 * N + 3), "i"(IMM) : "memory"); }
template <int N, bool Z> DI void mfma32a(v16f& acc, v4u b) {
  if constexpr (Z) asm volatile("v_mfma_f32_32x32x16_bf16 %0, a[%c2:%c3], %1, 0" : "=&v"(acc) : "v"(b), "i"(4 * N), "i"(4 * N + 3));
  else asm volatile("v_mfma_f32_32x32x16_bf16 %0, a[%c2:%c3], %1, %0" : "+v"(acc) : "v"(b), "i"(4 * N), "i"(4 * N + 3));
}
template <bool Z> DI void mfma32(v16f& acc, v4u a, v4u b) {
  if constexpr (Z) asm volatile("v_mfma_f32_32x32x16_bf16 %0, %1, %2, 0" : "=&v"(acc) : "v"(a), "v"(b));
  else asm volatile("v_mfma_f32_32x32x16_bf16 %0, %1, %2, %0" : "+v"(acc) : "v"(a), "v"(b));
}
template <int N, int M, bool Z> DI void mfma32aa(v16f& acc) {
  if constexpr (Z) asm volatile("v_mfma_f32_32x32x16_bf16 %0, a[%c1:%c2], a[%c3:%c4], 0" : "=&v"(acc) : "i"(4 * N), "i"(4 * N + 3), "i"(4 * M), "i"(4 * M + 3));
  else asm volatile("v_mfma_f32_32x32x16_bf16 %0, a[%c1:%c2], a[%c3:%c4], %0" : "+v"(acc) : "i"(4 * N), "i"(4 * N + 3), "i"(4 * M), "i"(4 * M + 3));
}
DI float dot2(u32 a, u32 b, float c) { return __builtin_amdgcn_fdot2_f32_bf16(__builtin_bit_cast(v2bf, a), __builtin_bit_cast(v2bf, b), c, false); }
DI float f_(u32 x) { return __builtin_bit_cast(float, x); }
DI u32 u_(float x) { return __builtin_bit_cast(u32, x); }
DI float fold32(float a, float b) { auto r = __builtin_amdgcn_permlane32_swap(u_(a), u_(b), false, false); return f_(r[0]) + f_(r[1]); }
DI float fold16(float a, float b) { auto r = __builtin_amdgcn_permlane16_swap(u_(a), u_(b), false, false); return f_(r[0]) + f_(r[1]); }
template <bool Z> DI void mfma16(v4f& acc, v4u a, v4u b) {
  if constexpr (Z) asm volatile("v_mfma_f32_16x16x32_bf16 %0, %1, %2, 0" : "=&v"(acc) : "v"(a), "v"(b));
  else asm volatile("v_mfma_f32_16x16x32_bf16 %0, %1, %2, %0" : "+v"(acc) : "v"(a), "v"(b));
}
template <int I, int N, class Fn> DI void sfor(Fn&& f) { if constexpr (I < N) { f(std::integral_constant<int, I>{}); sfor<I + 1, N>(f); } }
DI void poll(const char* p, u64 want) {
  for (u32 sp = 0; sp < SPIN_CAP; ++sp) { if (__all(__hip_atomic_load((GAS u64*)p, RLX_AGENT) == want)) break; }
  ORDER();
}
#define STAMP(n)
#define STAMPC(n)
__device__ constexpr int CP[36] = {0, 0,1,1, 0,2,1,2,2, 0,3,1,3,2,3,3, 0,4,1,4,2,4,3,4, 0,5,1,5,2,5, 0,6,1,6, 0,7};
__device__ constexpr int CQ[36] = {0, 1,0,1, 2,0,2,1,2, 3,0,3,1,3,2,3, 4,0,4,1,4,2,4,3, 5,0,5,1,5,2, 6,0,6,1, 7,0};
constexpr bool first_of(int m) { for (int x = 0; x < m; ++x) if (CP[x] + CQ[x] == CP[m] + CQ[m]) return false; return true; }

template <int MASK>
__global__ __launch_bounds__(256, 1) void mega(Params P) {
  constexpr bool FUSED = MASK == 31;
  extern __shared__ __attribute__((aligned(16))) char smem_[];
  asm volatile("" ::: "a0", "a255");
  const int b = blockIdx.x, t = threadIdx.x, lane = t & 63, w = __builtin_amdgcn_readfirstlane(t >> 6);
  const int j = b & 7, i = b >> 3;
  const rsrc_t rws = mkrs(P.ws, WS_BYTES);
  const v4u sws = mksrd(P.ws, WS_BYTES);
  const int g = lane >> 4, i16 = lane & 15, q4 = i16 >> 2, p4 = lane & 3;
  const bool is_stat = i >= 28;
  const int mt = 4 * (j >> 1) + (i & 3), nt = 7 * (j & 1) + (i >> 2);
  const int rb = 4 * j + (i - 28), tvt = rb * 4 + w;
  const int mt2 = 4 * j + (i & 3), dt = i >> 2;
  STAMP(0); STAMPC(24);
  u32 pf = 0;
  if constexpr (PF_BYTES > 0) {
    u64 pc; asm volatile("s_getpc_b64 %0" : "=s"(pc));
    const rsrc_t rc = mkrs((const void*)(pc & ~127ull), PF_BYTES);
    pf = __builtin_amdgcn_raw_buffer_load_b32(rc, t * 128, 0, 0) ^ __builtin_amdgcn_raw_buffer_load_b32(rc, t * 128 + 32768, 0, 0); }
  u64 tag = 0, ep = 0;
  v4u sk0, sk1, sk2, sk3;
  auto load_sk = [&]() {
    const rsrc_t r = mkrs(P.sk, D * S * 2);
    const int tt = t & 127; const u32 o = (64 * j + 2 * i + (t >> 7)) * 4096 + tt * 16;
    sk0 = ld16(r, tt ? o - 16 : 0x80000000u); sk1 = ld16(r, o); sk2 = ld16(r, o + 2032); sk3 = ld16(r, o + 2048);
  };
  auto build_kc = [&]() {
    const u32 base = KC_L + (t >> 7) * KC_CH + (1 + (t & 127)) * 16;
    auto one = [&](v4u a, v4u c, u32 ad) {
      u32 d[8] = {a.x, a.y, a.z, a.w, c.x, c.y, c.z, c.w}, s[7];
#pragma unroll
      for (int m = 0; m < 7; ++m) s[m] = __builtin_amdgcn_alignbit(d[m + 1], d[m], 16);
#pragma unroll
      for (int r = 0; r < 8; ++r) { v4u o; if (r & 1) { const int m = (r + 1) / 2; o = v4u{d[m], d[m + 1], d[m + 2], d[m + 3]}; } else { const int m = r / 2; o = v4u{s[m], s[m + 1], s[m + 2], s[m + 3]}; }
        sts16(ad + r * KC_CS, o); }
    };
    one(sk0, sk1, base); one(sk2, sk3, base + 2048);
    const v4u z = {0, 0, 0, 0};
    if (t < 16) sts16(KC_L + (t >> 3) * KC_CH + (t & 7) * KC_CS, z);
    if (t >= 64 && t < 128 && (t & 31) < 30) sts16(WL_L + ((t >> 5) & 1) * WL_CH + 4096 + (t & 31) * 16, z);
  };

  if constexpr (MASK & 1) {
    auto front = [&](auto rolec) { constexpr int R = rolec;
      const int c8 = t & 7, rp = t >> 3;
      v4u kk[5], vv[2], sh[4];
      { const rsrc_t rk = mkrs(P.k, S * D * 2), rv = mkrs(P.v, S * D * 2), rs = mkrs(P.sh, D * 8);
        const int r0 = 64 * i + 2 * rp; const u32 col = (64 * j + 8 * c8) * 2;
#pragma unroll
        for (int m = 0; m < 5; ++m) kk[m] = ld16(rk, (u32)((r0 - 3 + m) * 1024) + col);
        vv[0] = ld16(rv, r0 * 1024 + col); vv[1] = ld16(rv, r0 * 1024 + col + 1024);
#pragma unroll
        for (int m = 0; m < 4; ++m) sh[m] = ld16(rs, col * 4 + 16 * m); }
      if constexpr (FUSED) { ep = __builtin_bit_cast(u64, ld8c(rws, 0, EPOCH_OFF)); load_sk(); }
      v4u sa_[16], sb_[8]; u32 cr[2];
      const bool do_mod = b < 192, do_wd = b < 224, do_w = b >= 32;
      const int cg = b % 48, ksp = b / 48, fb = b >> 3, wn = (b - 32) >> 4, kb = b & 15;
      const u32 l8 = (lane >> 3) * 1024 + (lane & 7) * 16;
      if constexpr (R == 1) { const rsrc_t rq = mkrs(P.q, S * D * 2);
#pragma unroll
        for (int r = 0; r < 8; ++r) sa_[r] = ld16(rq, lane * 16, (8 * b + r) * 1024);
#pragma unroll
        for (int n = 0; n < 8; ++n) sb_[n] = ld16(rq, l8, (64 * i + 8 * n) * 1024 + 128 * j); }
      if constexpr (R == 2) { const rsrc_t rm = mkrs(P.wm, do_mod ? D * 6 * D * 2 : 0), rc = mkrs(P.c, D * 2), rd = mkrs(P.wd, do_wd ? F * D * 2 : 0);
        cr[0] = __builtin_amdgcn_raw_buffer_load_b16(rc, (128 * ksp + lane) * 2, 0, 0); cr[1] = __builtin_amdgcn_raw_buffer_load_b16(rc, (128 * ksp + 64 + lane) * 2, 0, 0);
#pragma unroll
        for (int n = 0; n < 16; ++n) sa_[n] = ld16(rm, (lane >> 3) * 6144 + (lane & 7) * 16, (128 * ksp + 8 * n) * 6144 + 128 * cg);
#pragma unroll
        for (int n = 0; n < 8; ++n) sb_[n] = ld16(rd, l8, (64 * fb + 8 * n) * 1024 + 128 * j); }
      if constexpr (R == 3) { const rsrc_t rg = mkrs(P.wg, do_w ? D * F * 2 : 0), ru = mkrs(P.wu, do_w ? D * F * 2 : 0);
#pragma unroll
        for (int n = 0; n < 8; ++n) { const u32 so = do_w ? (32 * kb + 4 * n) * 3584 + 256 * wn : 0; sa_[n] = ld16(rg, g * 3584 + i16 * 16, so); sa_[8 + n] = ld16(ru, g * 3584 + i16 * 16, so); } }
      SB();
      if constexpr (FUSED) tag = (((u64)(u32)__builtin_amdgcn_readfirstlane((u32)(ep >> 32)) << 32) | (u32)__builtin_amdgcn_readfirstlane((u32)ep)) + 1;
      asm volatile("" ::"v"(pf));
      if constexpr (R == 0) { if (lane == 0) *(LAS u32*)MISC_L = 0;
        if constexpr (FUSED) if (i < 2 && lane < 16)
          __hip_atomic_store((GAS u64*)(P.ws + (lane < 8 ? CNT0_OFF : CNTB_OFF) + ((lane & 7) * 16 + 2 * j + i) * 128), tag << 8, RLX_AGENT); }
      { v4u o0, o1;
#pragma unroll
        for (int e = 0; e < 4; ++e) {
          const v2f t0 = {lo16(sh[e].x), lo16(sh[e].z)}, t1 = {hi16(sh[e].x), hi16(sh[e].z)}, t2 = {lo16(sh[e].y), lo16(sh[e].w)}, t3 = {hi16(sh[e].y), hi16(sh[e].w)};
          const v2f k0 = unpk(kk[0][e]), k1 = unpk(kk[1][e]), k2 = unpk(kk[2][e]), k3 = unpk(kk[3][e]), k4 = unpk(kk[4][e]);
          o0[e] = pk((k0 * t3 + k1 * t2 + k2 * t1 + k3 * t0) * unpk(vv[0][e]));
          o1[e] = pk((k1 * t3 + k2 * t2 + k3 * t1 + k4 * t0) * unpk(vv[1][e]));
        }
        const int f = (rp & 1) | (((rp >> 2) & 1) << 1);
        const u32 ad = KV_L + rp * 256 + (((c8 >> 1) ^ f) << 5) + ((c8 & 1) << 4);
        sts16(ad, o0); sts16(ad + 128, o1); }
      STAMP(1);
      BAR();
      auto transpose64 = [&](u32 tile, bool rev, u32 dst, u32 pos0) {
        v4s h[4][2][2];
#pragma unroll
        for (int cb = 0; cb < 4; ++cb)
#pragma unroll
          for (int it = 0; it < 2; ++it)
#pragma unroll
            for (int tt = 0; tt < 2; ++tt) { const int x = 8 * (4 * it + g) + 4 * tt + q4, r = rev ? 63 - x : x, f = ((r >> 1) & 1) | (((r >> 3) & 1) << 1);
              h[cb][it][tt] = trr(tile + r * 128 + ((cb ^ f) << 5) + p4 * 8); }
        SB();
#pragma unroll
        for (int cb = 0; cb < 4; ++cb)
#pragma unroll
          for (int it = 0; it < 2; ++it) st16c(cat(h[cb][it][0], h[cb][it][1]), rws, i16 * 4096 + g * 16 + it * 64, dst + (64 * j + 16 * cb) * 4096 + pos0 * 2);
      };
      if constexpr (R == 0) {
        transpose64(KV_L, true, WT_OFF, 1984 - 64 * i);
        if constexpr (FUSED) { DRAIN(); if (lane == 0) __hip_atomic_store((GAS u64*)(P.ws + FLAGA_OFF + (j * 32 + i) * 128), tag, RLX_AGENT); }
        STAMP(2);
        if constexpr (FUSED) build_kc();
      } else {
        if constexpr (FUSED) build_kc();
        STAMP(2);
        const u32 sw64 = (lane >> 3) * 128 + (((lane & 7) ^ ((g & 1) << 2)) << 4);
        if constexpr (R == 1) {
          float sm[8], sq[8];
#pragma unroll
          for (int r = 0; r < 8; ++r) { sm[r] = 0; sq[r] = 0;
#pragma unroll
            for (int e = 0; e < 4; ++e) { sm[r] = dot2(sa_[r][e], 0x3f803f80u, sm[r]); sq[r] = dot2(sa_[r][e], sa_[r][e], sq[r]); } }
          float m = 0, v = 0;
          { const float a0 = fold16(fold32(sm[0], sm[4]), fold32(sm[2], sm[6])), a1 = fold16(fold32(sm[1], sm[5]), fold32(sm[3], sm[7]));
            const float b0 = fold16(fold32(sq[0], sq[4]), fold32(sq[2], sq[6])), b1 = fold16(fold32(sq[1], sq[5]), fold32(sq[3], sq[7]));
            const bool od = lane & 8;
            float c0 = dppadd<0x128>(a0), c1 = dppadd<0x128>(a1), d0 = dppadd<0x128>(b0), d1 = dppadd<0x128>(b1);
            asm volatile("" : "+v"(c0), "+v"(c1), "+v"(d0), "+v"(d1));
            m = dppadd<0x141>(dppadd<0x4E>(dppadd<0xB1>(od ? c1 : c0))) * (1.f / D);
            v = dppadd<0x141>(dppadd<0x4E>(dppadd<0xB1>(od ? d1 : d0))) * (1.f / D);
            asm volatile("" : "+v"(m), "+v"(v)); }
          const v2f o = {m, rsqrtf(fmaxf(v - m * m, 0.f) + EPS)};
          if ((lane & 7) == 0) __builtin_amdgcn_raw_buffer_store_b64(__builtin_bit_cast(v2u, o), rws, g * 4096 + (lane & 8), ST1_OFF + b * 16, 16);
#pragma unroll
          for (int n = 0; n < 8; ++n) { const int f = (g & 1) | ((n & 1) << 1); sts16(QL_L + n * 1024 + (lane >> 3) * 128 + ((((lane & 7) >> 1) ^ f) << 5) + ((lane & 1) << 4), sb_[n]); }
          transpose64(QL_L, false, QT_OFF, 64 * i);
        }
        if constexpr (R == 2) { const int h = lane >> 5, blk = g & 1;
#pragma unroll
          for (int n = 0; n < 2; ++n) { const float x = lo16(cr[n]); *(LAS u16*)(SV_L + n * 128 + lane * 2) = (u16)pk(x / (1.f + __expf(-x)), 0.f); }
#pragma unroll
          for (int n = 0; n < 16; ++n) sts16(WM_L + n * 1024 + sw64, sa_[n]);
          v4u fa[8]; v4s fw[8][2][2]; v16f ma[2];
#pragma unroll
          for (int ks = 0; ks < 8; ++ks) { fa[ks] = lds16(SV_L + ks * 32 + h * 16);
#pragma unroll
            for (int ct = 0; ct < 2; ++ct)
#pragma unroll
              for (int tt = 0; tt < 2; ++tt) fw[ks][ct][tt] = trr(WM_L + (16 * ks + 8 * h + 4 * tt + q4) * 128 + ((ct ^ (q4 >> 1)) << 6) + blk * 32 + p4 * 8); }
          SB();
          sfor<0, 16>([&](auto c) { constexpr int ks = c >> 1, ct = c & 1; mfma32<ks == 0>(ma[ct], fa[ks], cat(fw[ks][ct][0], fw[ks][ct][1])); });
#pragma unroll
          for (int n = 0; n < 8; ++n) sts16(WDS_L + n * 1024 + sw64, sb_[n]);
          asm volatile("s_nop 7" : "+v"(ma[0]), "+v"(ma[1])::"memory");
          { const float m0 = ma[0][0], m1 = ma[1][0]; if (do_mod) __builtin_amdgcn_raw_buffer_store_b32(u_(h ? m1 : m0), rws, lane * 4, MODP_OFF + (ksp * 3072 + 64 * cg) * 4, 16); }
          if (do_wd) { v4s x[4][2][2];
#pragma unroll
            for (int ks = 0; ks < 4; ++ks)
#pragma unroll
              for (int dl = 0; dl < 2; ++dl)
#pragma unroll
                for (int tt = 0; tt < 2; ++tt) x[ks][dl][tt] = trr(WDS_L + (16 * ks + 8 * tt + 4 * h + q4) * 128 + ((dl ^ (q4 >> 1)) << 6) + blk * 32 + p4 * 8);
            SB();
#pragma unroll
            for (int ks = 0; ks < 4; ++ks)
#pragma unroll
              for (int dl = 0; dl < 2; ++dl) st16c(cat(x[ks][dl][0], x[ks][dl][1]), rws, lane * 16 + dl * 1024 + (ks & 1) * 2048, WD_OFF + (j * 112 + 4 * fb + (ks & 2)) * 2048); } }
        if constexpr (R == 3) if (do_w) { const int mat = g & 1, h = g >> 1;
#pragma unroll
          for (int n = 0; n < 16; ++n) sts16(WS_L + n * 1024 + g * 256 + ((((i16 >> 1) ^ g ^ ((n >> 3) << 2)) << 5) | ((i16 & 1) << 4)), sa_[n]);
          v4s x[8][2][2];
#pragma unroll
          for (int ft = 0; ft < 8; ++ft)
#pragma unroll
            for (int ks = 0; ks < 2; ++ks)
#pragma unroll
              for (int tt = 0; tt < 2; ++tt) x[ft][ks][tt] = trr(WS_L + mat * 8192 + (16 * ks + 8 * h + 4 * tt + q4) * 256 + ((ft ^ (q4 | (mat << 2))) << 5) + p4 * 8);
          SB();
#pragma unroll
          for (int ft = 0; ft < 8; ++ft)
#pragma unroll
            for (int ks = 0; ks < 2; ++ks) st16c(cat(x[ft][ks][0], x[ft][ks][1]), rws, lane * 16 + ks * 2048 + (ft & 1) * 1024, WG_OFF + (wn * 4 + (ft >> 1)) * 65536 + kb * 4096); }
        if constexpr (FUSED) {
          const char* ini = P.ws + FLAGA_OFF + (j * 32 + (i & 1)) * 128;
          const u64 f0 = __hip_atomic_load((GAS u64*)ini, RLX_AGENT);
          DRAIN();
          u32 old = 0; if (lane == 0) old = __hip_atomic_fetch_add((LAS u32*)MISC_L, 1u, __ATOMIC_RELAXED, __HIP_MEMORY_SCOPE_WORKGROUP);
          if (__builtin_amdgcn_readfirstlane(old) == 2) { if (!__all(f0 == tag)) poll(ini, tag);
            if (lane < 8) __hip_atomic_fetch_add((GAS u64*)(P.ws + CNT0_OFF + (lane * 16 + 2 * j + (i & 1)) * 128), 1ull, RLX_AGENT); } }
        STAMP(3);
      }
    };
    if (w == 0) front(std::integral_constant<int, 0>{}); else if (w == 1) front(std::integral_constant<int, 1>{});
    else if (w == 2) front(std::integral_constant<int, 2>{}); else front(std::integral_constant<int, 3>{});
  }

  if constexpr (MASK & 2) {
    if constexpr (!FUSED) { load_sk(); build_kc(); }
    if (w == 0) {
      if constexpr (FUSED) poll(P.ws + FLAGA_OFF + (j * 32 + (lane & 31)) * 128, tag);
      STAMP(3);
#pragma unroll
      for (int n = 0; n < 8; ++n) gldsc(sws, lane * 16, WT_OFF + (64 * j + 2 * i + (n >> 2)) * 4096 + (n & 3) * 1024, WL_L + (n >> 2) * WL_CH + (n & 3) * 1024);
      DRAIN();
    }
    STAMP(4);
    BAR();
    STAMP(5);
    const int ch = w >> 1, kh = w & 1, d = 64 * j + 2 * i + ch;
    const u32 aA = KC_L + ch * KC_CH + (i16 & 7) * KC_CS + ((i16 >> 3) + g) * 16 + kh * 256;
    const u32 aB = WL_L + ch * WL_CH + 480 - 32 * i16 + 16 * g + kh * 256;
    v4u fa[2][8], fb_[2][8]; v4f acc[8];
    v4u qv[2], st[2][4]; u32 mraw = 0, draw = 0, graw = 0;
#pragma unroll
    for (int m = 0; m < 8; ++m) { fa[0][m] = lds16(aA + 512 * m); fb_[0][m] = lds16(aB + 512 * (7 - m)); }
    SB();
    sfor<0, 4>([&](auto sc) { constexpr int s = sc;
      if constexpr (s == 2) {
        if constexpr (FUSED) poll(P.ws + CNT0_OFF + (j * 16 + i16) * 128, (tag << 8) + 16);
        STAMP(6); STAMPC(25);
#pragma unroll
        for (int pr = 0; pr < 2; ++pr) { const u32 s0 = 256 * (4 * kh + 2 * pr + (g & 1)) + 16 * i16 + 8 * (g >> 1);
          qv[pr] = ld16c(rws, s0 * 2, QT_OFF + d * 4096);
#pragma unroll
          for (int m = 0; m < 4; ++m) st[pr][m] = ld16c(rws, s0 * 2, ST1_OFF + m * 4096); }
        mraw = ld4c(rws, (p4 * 3072 + (q4 == 3 ? 4 : q4) * 512) * 4, MODP_OFF + d * 4);
        draw = __builtin_amdgcn_raw_buffer_load_b16(mkrs(P.Dv, D * 2), 0, d * 2, 0);
        if constexpr (FUSED) graw = ld4c(rws, (p4 * 3072 + (t >> 2)) * 4, MODP_OFF + (5 * 512 + 64 * dt) * 4);
        SB();
      }
      sfor<0, 36>([&](auto mc) { constexpr int m = mc;
        mfma16<s == 0 && first_of(m)>(acc[CP[m] + CQ[m]], fa[s & 1][CP[m]], fb_[s & 1][CQ[m]]);
        if constexpr (s < 3 && (m & 1) == 0 && m < 32) { constexpr int x = m >> 2;
          if constexpr (m & 2) fb_[(s + 1) & 1][x] = lds16(aB + 512 * (7 - x) + 64 * (s + 1)); else fa[(s + 1) & 1][x] = lds16(aA + 512 * x + 64 * (s + 1)); }
        SB(); });
    });
    asm volatile("s_nop 7\n\ts_nop 1" : "+v"(acc[0]), "+v"(acc[1]), "+v"(acc[2]), "+v"(acc[3]), "+v"(acc[4]), "+v"(acc[5]), "+v"(acc[6]), "+v"(acc[7])::"memory");
    STAMP(7); STAMPC(26);
    v4f y[4];
#pragma unroll
    for (int m = 0; m < 4; ++m) { sts16(RED_L + w * 4096 + m * 1024 + lane * 16, __builtin_bit_cast(v4u, kh ? acc[m] : acc[4 + m])); y[m] = kh ? acc[4 + m] : acc[m]; }
    BAR();
#pragma unroll
    for (int m = 0; m < 4; ++m) y[m] += __builtin_bit_cast(v4f, lds16(RED_L + (w ^ 1) * 4096 + m * 1024 + lane * 16));
    if constexpr (FUSED) { const float gq = rbf(dppadd<0x4E>(dppadd<0xB1>(f_(graw)))); if (p4 == 0) *(LAS float*)(GB_L + (t >> 2) * 4) = gq; }
    const float mq = rbf(dppadd<0x4E>(dppadd<0xB1>(__builtin_bit_cast(float, mraw))));
    auto rl = [&](int l) { return __builtin_bit_cast(float, __builtin_amdgcn_readlane(__builtin_bit_cast(int, mq), l)); };
    const float sa = rl(0), a1 = rbf(1.f + rl(4)), ga = rl(8), sg = rbf(1.f + rl(12)), dv = lo16(draw);
#pragma unroll
    for (int pr = 0; pr < 2; ++pr) { const u32 s0 = 256 * (4 * kh + 2 * pr + (g & 1)) + 16 * i16 + 8 * (g >> 1);
      float yy[8];
#pragma unroll
      for (int e = 0; e < 4; ++e) { const float ya = y[2 * pr][e], yb = y[2 * pr + 1][e]; auto r = __builtin_amdgcn_permlane16_swap(__builtin_bit_cast(u32, ya), __builtin_bit_cast(u32, yb), false, false);
        yy[e] = __builtin_bit_cast(float, (u32)r[0]); yy[4 + e] = __builtin_bit_cast(float, (u32)r[1]); }
      const v4u uw = lds16(WL_L + ch * WL_CH + (2040 - s0) * 2);
      v4u o1, o2;
#pragma unroll
      for (int e = 0; e < 4; ++e) { const v2f x = unpk(qv[pr][e]), u = {hi16(uw[3 - e]), lo16(uw[3 - e])}, yv = {yy[2 * e], yy[2 * e + 1]};
        const v4f sm = __builtin_bit_cast(v4f, st[pr][e]); const v2f mn = {sm.x, sm.z}, rs = {sm.y, sm.w};
        const v2f h = (x - mn) * rs * a1 + sa;
        o1[e] = pk(x + ga * (h * (yv + u * dv)));
        o2[e] = pk(unpk(o1[e]) * sg); }
      st16c(o1, rws, s0 * 2, X1_OFF + d * 4096); st16c(o2, rws, s0 * 2, X1S_OFF + d * 4096); }
    STAMP(8);
    if constexpr (FUSED) { DRAIN(); BAR();
      if (w == 0 && lane < 8) __hip_atomic_fetch_add((GAS u64*)(P.ws + CNTB_OFF + (lane * 16 + 2 * j + (i & 1)) * 128), 1ull, RLX_AGENT); }
    STAMP(9);
  }

  const u32 wdso = WD_OFF + ((dt * 112 + 28 * w) * 2) * 1024;
  auto res_tile = [&]() {
    const int cs = (lane & 7) ^ (((lane >> 4) & 1) << 2);
#pragma unroll
    for (int n = 0; n < 2; ++n) gldsc(sws, (lane >> 3) * 4096 + cs * 16, X1_OFF + (64 * dt + 16 * w + 8 * n) * 4096 + 128 * mt2, X1L_L + (2 * w + n) * 1024);
  };
  if constexpr (MASK & 4) if (is_stat) {
    { v2u mp[8];
#pragma unroll
      for (int m = 0; m < 4; ++m) { mp[m] = ld8c(rws, t * 8, MODP_OFF + (m * 3072 + 4 * 512) * 4); mp[4 + m] = ld8c(rws, t * 8, MODP_OFF + (m * 3072 + 3 * 512) * 4); }
      SB();
      if (tvt < 112) sfor<0, 32>([&](auto c) { constexpr int ks = c; lda<ks, (ks & 1) * 2048>(sws, lane * 16, WG_OFF + (tvt >> 1) * 65536 + (tvt & 1) * 1024 + (ks >> 1) * 4096); });
      v2f cb = {0, 0}, sb = {0, 0};
#pragma unroll
      for (int m = 0; m < 4; ++m) { cb += __builtin_bit_cast(v2f, mp[m]); sb += __builtin_bit_cast(v2f, mp[4 + m]); }
      *(LAS u32*)(SIG_L + t * 4) = pk(unpk(pk(cb)) + 1.f); *(LAS u32*)(SIG_L + 1024 + t * 4) = pk(sb); }
    DRAIN(); BAR();
    if (tvt < 112) { v16f a; v4u fs[32]; const u32 ad = SIG_L + (lane & 1) * 1024 + (lane >> 5) * 16;
#pragma unroll
      for (int ks = 0; ks < 32; ++ks) fs[ks] = lds16(ad + 32 * ks);
      SB();
      sfor<0, 32>([&](auto c) { constexpr int ks = c; mfma32a<ks, ks == 0>(a, fs[ks]); });
      asm volatile("s_nop 7\n\ts_nop 7" : "+v"(a)::"memory");
      if ((lane & 31) < 2) {
#pragma unroll
        for (int m = 0; m < 4; ++m) st16c(__builtin_bit_cast(v4u, v4f{a[4 * m], a[4 * m + 1], a[4 * m + 2], a[4 * m + 3]}), rws, (lane >> 5) * 128 + (lane & 1) * 64 + m * 16, TV_OFF + tvt * 256); } }
    if constexpr (FUSED) { if (w == 0) poll(P.ws + CNTB_OFF + (j * 16 + i16) * 128, (tag << 8) + 16); BAR(); }
    STAMP(10);
    { const int cs = (lane & 7) ^ ((g & 1) << 2), h = lane >> 5;
      v4u one = {0x3f803f80u, 0x3f803f80u, 0x3f803f80u, 0x3f803f80u}; asm volatile("" : "+v"(one));
#pragma unroll
      for (int n = 0; n < 16; ++n) gldsc(sws, (lane >> 3) * 4096 + cs * 16, X1_OFF + (128 * w + 8 * n) * 4096 + 128 * rb, w * 16384 + n * 1024);
      v16f gs[2], gq[2]; v4s x[8][2][2];
      sfor<0, 2>([&](auto hc) { constexpr int hf = hc;
        WAITV(8 - 8 * hf);
#pragma unroll
        for (int ks = 4 * hf; ks < 4 * hf + 4; ++ks)
#pragma unroll
          for (int st = 0; st < 2; ++st)
#pragma unroll
            for (int tt = 0; tt < 2; ++tt) x[ks][st][tt] = trr(w * 16384 + (16 * ks + 8 * h + 4 * tt + q4) * 128 + ((st ^ (q4 >> 1)) << 6) + (g & 1) * 32 + p4 * 8);
        SB();
        sfor<8 * hf, 8 * hf + 8>([&](auto c) { constexpr int ks = c >> 1, st = c & 1; const v4u fr = cat(x[ks][st][0], x[ks][st][1]); mfma32<ks == 0>(gq[st], fr, fr); mfma32<ks == 0>(gs[st], one, fr); }); });
      asm volatile("s_nop 7\n\ts_nop 7" : "+v"(gs[0]), "+v"(gs[1]), "+v"(gq[0]), "+v"(gq[1])::"memory");
      const int dr = (lane & 3) + 4 * ((lane & 31) >> 3);
#pragma unroll
      for (int st = 0; st < 2; ++st) { float dg = gq[st][0];
#pragma unroll
        for (int r = 1; r < 16; ++r) { const float e = gq[st][r]; dg = dr == r ? e : dg; }
        const float s0 = gs[st][0];
        if (((lane >> 2) & 1) == h) *(LAS v2f*)(RED_L + w * 512 + (32 * st + (lane & 31)) * 8) = v2f{s0, dg}; }
      BAR();
      if (w == 0) { v2f a = {0, 0};
#pragma unroll
        for (int u = 0; u < 4; ++u) a += *(LAS v2f*)(RED_L + u * 512 + lane * 8);
        a *= (1.f / D);
        const v2f o = {a.x, rsqrtf(fmaxf(a.y - a.x * a.x, 0.f) + EPS)};
        __builtin_amdgcn_raw_buffer_store_b64(__builtin_bit_cast(v2u, o), rws, lane * 8, ST2_OFF + rb * 512, 16); } }
    STAMP(11);
    if constexpr (FUSED) { DRAIN(); BAR(); if (t == 0) __hip_atomic_store((GAS u64*)(P.ws + FLAGS_OFF + rb * 128), tag, RLX_AGENT);
      STAMP(12);
      res_tile();
      sfor<0, 56>([&](auto c) { constexpr int n = c; lda<n, (n & 3) * 1024>(sws, lane * 16, wdso + (n >> 2) * 4096); }); }
    STAMP(13);
  }

  if constexpr (MASK & 8) if (!is_stat) {
    const u32 wso = WG_OFF + (nt * 4 + w) * 65536;
    if (w != 0) asm volatile("s_barrier" ::: "memory");
    sfor<0, 32>([&](auto c) { constexpr int n = c; lda<n, (n & 3) * 1024>(sws, lane * 16, wso + (n >> 2) * 4096); });
    if (w == 0) { asm volatile("s_barrier" ::: "memory");
      if constexpr (FUSED) { poll(P.ws + CNTB_OFF + (j * 16 + i16) * 128, (tag << 8) + 16);
        if (b == 0 && lane == 0) __hip_atomic_store((GAS u64*)(P.ws + EPOCH_OFF), tag, RLX_AGENT); } }
    asm volatile("s_barrier" ::: "memory");
    STAMP(10);
    const u32 xo = (4 * w + g) * 4096 + ((i16 ^ ((g << 2) | w)) << 4);
    auto stageA = [&](int ks) { gldsc(sws, xo, X1S_OFF + ks * 65536 + mt * 256, A_L + ks * 4096 + w * 1024); };
#pragma unroll
    for (int ks = 0; ks < 8; ++ks) stageA(ks);
    u32 base[4][2];
#pragma unroll
    for (int c = 0; c < 4; ++c)
#pragma unroll
      for (int tt = 0; tt < 2; ++tt) { const u32 row = 8 * (lane >> 5) + 4 * tt + q4, chk = 4 * c + 2 * (g & 1) + (p4 >> 1);
        base[c][tt] = A_L + 256u * row + 16u * (chk ^ (((row & 3) << 2) | ((row >> 2) & 3))) + 8 * (p4 & 1); }
    v16f acc[2][4]; v4s f[2][4][2]; u64 fl = 0;
    asm volatile("s_waitcnt vmcnt(5)\n\ts_barrier" ::: "memory");
    STAMP(11); STAMPC(27);
#pragma unroll
    for (int c = 0; c < 4; ++c) { f[0][c][0] = trr(base[c][0]); f[0][c][1] = trr(base[c][1]); }
    const char* fsp = P.ws + FLAGS_OFF + (lane & 31) * 128;
    sfor<0, 32>([&](auto kc) { constexpr int ks = kc;
      if constexpr (ks == 2) asm volatile("s_waitcnt vmcnt(6)\n\ts_barrier" ::: "memory");
      if constexpr (ks == 7) asm volatile("s_waitcnt vmcnt(9)\n\ts_barrier" ::: "memory");
      if constexpr (ks == 19) asm volatile("s_waitcnt vmcnt(22)\n\ts_barrier" ::: "memory");
      else if constexpr (ks >= 16) WAITV(22);
      if constexpr (ks == 15) {
#pragma unroll
        for (int c = 0; c < 4; ++c) { base[c][0] += 65536; base[c][1] += 65536; asm volatile("" : "+v"(base[c][0]), "+v"(base[c][1])); } }
      if constexpr (ks == 8) STAMP(18);
      if constexpr (ks == 16) STAMP(19);
      if constexpr (ks == 22) STAMP(20);
      if constexpr (FUSED && ks == 22) asm volatile("global_load_dwordx2 %0, %1, off sc1" : "=v"(fl) : "v"(fsp) : "memory");
      if constexpr (ks == 26) {
        if constexpr (FUSED) { u32 n = SPIN_CAP;
          asm volatile("s_waitcnt vmcnt(12)\n"
                       "1:\n\tv_cmp_ne_u64 vcc, %3, %0\n\ts_nop 4\n\ts_cbranch_vccz 2f\n\ts_sub_u32 %1, %1, 1\n\ts_cbranch_scc1 2f\n\t"
                       "global_load_dwordx2 %0, %2, off sc1\n\ts_waitcnt vmcnt(0)\n\ts_branch 1b\n2:"
                       : "+v"(fl), "+s"(n) : "v"(fsp), "s"(tag) : "vcc", "scc", "memory"); }
        gldsc(sws, lane * 16, ST2_OFF + mt * 1024, EPI_L + w * 2048); gldsc(sws, lane * 16, TV_OFF + (nt * 8 + 2 * w) * 256, EPI_L + w * 2048 + 1024); }
      SB();
      sfor<0, 8>([&](auto mc) { constexpr int c = mc >> 1, T = mc & 1;
        mfma32a<2 * ks + T, ks == 0>(acc[T][c], cat(f[ks & 1][c][0], f[ks & 1][c][1]));
        if constexpr (ks < 31) f[(ks + 1) & 1][c][T] = trr(base[c][T] + ((ks + 1) * 4096 - (ks >= 15 ? 65536 : 0)));
        if constexpr (T == 1 && c < 3) { constexpr int z = 3 * ks + c;
          if constexpr (z < 24) stageA(8 + z);
          else if constexpr (z < 56) lda<z + 8, (z & 3) * 1024>(sws, lane * 16, wso + ((z + 8) >> 2) * 4096);
          else if constexpr (FUSED) lda<z - 56, (z & 3) * 1024>(sws, lane * 16, wdso + ((z - 56) >> 2) * 4096);
          else asm volatile("buffer_load_dword a0, %0, %1, 0 offen" ::"v"(lane), "s"(sws) : "memory"); }
        SB(); });
    });
    asm volatile("s_waitcnt vmcnt(18)\n\ts_nop 7\n\ts_nop 7" : "+v"(acc[0][0]), "+v"(acc[0][1]), "+v"(acc[0][2]), "+v"(acc[0][3]), "+v"(acc[1][0]), "+v"(acc[1][1]), "+v"(acc[1][2]), "+v"(acc[1][3])::"memory");
    STAMP(12); STAMPC(28);
    v2f mr[4];
#pragma unroll
    for (int c = 0; c < 4; ++c) { const v2f s2 = *(LAS v2f*)(EPI_L + w * 2048 + (32 * c + (lane & 31)) * 8); mr[c] = v2f{s2.y, -s2.x * s2.y}; }
#pragma unroll
    for (int T = 0; T < 2; ++T) { v4f t1[4], t2[4];
#pragma unroll
      for (int m = 0; m < 4; ++m) { const u32 ad = EPI_L + w * 2048 + 1024 + T * 256 + (lane >> 5) * 128 + m * 16; t1[m] = __builtin_bit_cast(v4f, lds16(ad)); t2[m] = __builtin_bit_cast(v4f, lds16(ad + 64)); }
#pragma unroll
      for (int c = 0; c < 4; ++c) { v4u o;
#pragma unroll
        for (int e = 0; e < 4; ++e) { const int r = 2 * e;
          const v2f ag = {acc[T][c][r], acc[T][c][r + 1]}, au = {acc[T][c][8 + r], acc[T][c][9 + r]};
          const v2f g1 = {t1[r >> 2][r & 3], t1[r >> 2][(r & 3) + 1]}, g2 = {t2[r >> 2][r & 3], t2[r >> 2][(r & 3) + 1]};
          const v2f u1 = {t1[2 + (r >> 2)][r & 3], t1[2 + (r >> 2)][(r & 3) + 1]}, u2 = {t2[2 + (r >> 2)][r & 3], t2[2 + (r >> 2)][(r & 3) + 1]};
          o[e] = pk((ag * mr[c].x + (g1 * mr[c].y + g2)) * (au * mr[c].x + (u1 * mr[c].y + u2))); }
        st16c(o, rws, lane * 16, HH_OFF + ((4 * mt + c) * 112 + 8 * nt + 2 * w + T) * 1024); } }
    STAMP(13);
    if constexpr (FUSED) { DRAIN(); if (lane == 0) __hip_atomic_store((GAS u64*)(P.ws + FLAGC_OFF + (mt * 56 + nt * 4 + w) * 128), tag, RLX_AGENT);
      res_tile();
      sfor<40, 56>([&](auto c) { constexpr int n = c; lda<n, (n & 3) * 1024>(sws, lane * 16, wdso + (n >> 2) * 4096); }); }
  }

  if constexpr (MASK & 16) {
    if constexpr (!FUSED) { res_tile(); sfor<0, 56>([&](auto c) { constexpr int n = c; lda<n, (n & 3) * 1024>(sws, lane * 16, wdso + (n >> 2) * 4096); }); }
    if constexpr (!FUSED) { const float gq = dppadd<0x4E>(dppadd<0xB1>(__builtin_bit_cast(float, ld4c(rws, (p4 * 3072 + (t >> 2)) * 4, MODP_OFF + (5 * 512 + 64 * dt) * 4))));
      if (p4 == 0) *(LAS float*)(GB_L + (t >> 2) * 4) = rbf(gq); }
    BAR();
    STAMP(14);
    if constexpr (FUSED) poll(P.ws + FLAGC_OFF + ((mt2 >> 1) * 56 + 14 * w + (i16 < 14 ? i16 : 13)) * 128, tag);
    DRAIN();
    STAMP(15);
    constexpr int WIN = 20;
    v4u hf[WIN][2]; v16f acc[2][2];
    const u32 hso = HH_OFF + (2 * mt2 * 112 + 28 * w) * 1024;
    sfor<0, 8>([&](auto c) { constexpr int n = c; lda<56 + n, (n >> 1) * 1024>(sws, lane * 16, hso + (n & 1) * 112 * 1024); });
    auto ldh = [&](int l) { hf[(l - 4) % WIN][0] = ld16c(rws, lane * 16 + (l & 3) * 1024, hso + (l >> 2) * 4096); hf[(l - 4) % WIN][1] = ld16c(rws, lane * 16 + (l & 3) * 1024, hso + 112 * 1024 + (l >> 2) * 4096); };
#pragma unroll
    for (int l = 4; l < 4 + WIN; ++l) ldh(l);
    SB();
    sfor<0, 28>([&](auto lc) { constexpr int l = lc;
      if constexpr (l < 4) { WAITV(2 * WIN + 6 - 2 * l);
        mfma32aa<2 * l, 56 + 2 * l, l == 0>(acc[0][0]); mfma32aa<2 * l + 1, 56 + 2 * l, l == 0>(acc[1][0]);
        mfma32aa<2 * l, 57 + 2 * l, l == 0>(acc[0][1]); mfma32aa<2 * l + 1, 57 + 2 * l, l == 0>(acc[1][1]);
      } else {
        mfma32a<2 * l, false>(acc[0][0], hf[(l - 4) % WIN][0]); mfma32a<2 * l + 1, false>(acc[1][0], hf[(l - 4) % WIN][0]);
        mfma32a<2 * l, false>(acc[0][1], hf[(l - 4) % WIN][1]); mfma32a<2 * l + 1, false>(acc[1][1], hf[(l - 4) % WIN][1]);
        if constexpr (l + WIN < 28) ldh(l + WIN); }
      SB(); });
    asm volatile("s_nop 7\n\ts_nop 7" : "+v"(acc[0][0]), "+v"(acc[0][1]), "+v"(acc[1][0]), "+v"(acc[1][1])::"memory");
    STAMP(16);
#pragma unroll
    for (int o = 0; o < 4; ++o) if (o != w) {
#pragma unroll
      for (int m = 0; m < 4; ++m) { const v16f& a = acc[o >> 1][o & 1]; sts16((o * 4 + w) * 4096 + m * 1024 + lane * 16, __builtin_bit_cast(v4u, v4f{a[4 * m], a[4 * m + 1], a[4 * m + 2], a[4 * m + 3]})); } }
    v16f r = (w & 2) ? ((w & 1) ? acc[1][1] : acc[1][0]) : ((w & 1) ? acc[0][1] : acc[0][0]);
    BAR();
#pragma unroll
    for (int u = 1; u < 4; ++u)
#pragma unroll
      for (int m = 0; m < 4; ++m) { const v4f x = __builtin_bit_cast(v4f, lds16((w * 4 + (w ^ u)) * 4096 + m * 1024 + lane * 16));
        r[4 * m] += x.x; r[4 * m + 1] += x.y; r[4 * m + 2] += x.z; r[4 * m + 3] += x.w; }
    const int dl = w >> 1, sl = w & 1, h = lane >> 5;
    v2u o[4];
#pragma unroll
    for (int G = 0; G < 4; ++G) { const v2u xr = __builtin_bit_cast(v2u, trr(X1L_L + (32 * dl + 8 * G + 4 * h + q4) * 128 + ((sl ^ (q4 >> 1)) << 6) + (g & 1) * 32 + p4 * 8));
      const v4f gb = __builtin_bit_cast(v4f, lds16(GB_L + (32 * dl + 8 * G + 4 * h) * 4));
      o[G].x = pk(unpk(xr.x) + v2f{gb.x, gb.y} * v2f{r[4 * G], r[4 * G + 1]}); o[G].y = pk(unpk(xr.y) + v2f{gb.z, gb.w} * v2f{r[4 * G + 2], r[4 * G + 3]}); }
    const rsrc_t ro = mkrs(P.out, S * D * 2);
#pragma unroll
    for (int G = 0; G < 4; G += 2) { auto r0 = __builtin_amdgcn_permlane32_swap(o[G].x, o[G + 1].x, false, false); auto r1 = __builtin_amdgcn_permlane32_swap(o[G].y, o[G + 1].y, false, false);
      st16(v4u{(u32)r0[0], (u32)r1[0], (u32)r0[1], (u32)r1[1]}, ro, (32 * sl + (lane & 31)) * 1024 + (8 * G + 8 * h) * 2, 64 * mt2 * 1024 + (64 * dt + 32 * dl) * 2); }
    STAMP(17); STAMPC(29);
  }
}

template <int MASK> static void launch(const Params& p, hipStream_t st) {
  hipFuncSetAttribute((const void*)mega<MASK>, hipFuncAttributeMaxDynamicSharedMemorySize, LDS_BYTES);
  mega<MASK><<<256, 256, LDS_BYTES, st>>>(p);
}
extern "C" void kernel_launch(void* const* d_in, const int* in_sizes, int n_in, void* d_out, int out_size, void* d_ws, size_t ws_size, hipStream_t stream) {
  Params p;
  p.q = (const u16*)d_in[0]; p.k = (const u16*)d_in[1]; p.v = (const u16*)d_in[2]; p.sh = (const u16*)d_in[3]; p.sk = (const u16*)d_in[4]; p.Dv = (const u16*)d_in[5];
  p.wg = (const u16*)d_in[7]; p.wu = (const u16*)d_in[8]; p.wd = (const u16*)d_in[9]; p.c = (const u16*)d_in[10]; p.wm = (const u16*)d_in[11];
  p.out = (u16*)d_out; p.ws = (char*)d_ws;
  launch<31>(p, stream);
}
```

```cpp
#include <hip/hip_runtime.h>
typedef unsigned short u16; typedef unsigned u32; typedef unsigned long long u64;
typedef u32 v2u __attribute__((ext_vector_type(2)));
typedef u32 v4u __attribute__((ext_vector_type(4)));
typedef float v2f __attribute__((ext_vector_type(2)));
typedef float v4f __attribute__((ext_vector_type(4)));
typedef float v16f __attribute__((ext_vector_type(16)));
typedef short v4s __attribute__((ext_vector_type(4)));
typedef __bf16 v2bf __attribute__((ext_vector_type(2)));
#define LAS __attribute__((address_space(3)))
#define GAS __attribute__((address_space(1)))
#define DI __device__ __forceinline__
#define SB() __builtin_amdgcn_sched_barrier(0)
#define RLX_AGENT __ATOMIC_RELAXED, __HIP_MEMORY_SCOPE_AGENT

constexpr int S = 2048, D = 512, F = 1792;
constexpr unsigned RS = 4224;
constexpr float EPS = 1e-5f;
constexpr u32 FS = 256;
constexpr u32 EPOCH_OFF = 0, FLAGA_OFF = 23u << 20  , CNT_OFF = FLAGA_OFF + 256 * FS  , FLAGC_OFF = CNT_OFF + 256 * FS  ;
constexpr u32 C0 = CNT_OFF, CB = CNT_OFF + 128 * FS;
constexpr u32 ST1_OFF = 0x60000, ST2_OFF = 0x80000, MODP_OFF = 0x68000, TV_OFF = 0x90000;
constexpr u32 WT_OFF = 1u << 20, QT_OFF = WT_OFF + 0x240000, X1_OFF = QT_OFF + 0x240000, X1S_OFF = X1_OFF + 0x240000, WG_OFF = 10u << 20, WD_OFF = 14u << 20, HH_OFF = 16u << 20, WS_BYTES = FLAGC_OFF + 1792 * FS;
constexpr u32 KC_L = 0, KC_CH = 33280, KC_CS = 4160;
constexpr u32 WL_L = 66560, WL_CH = 4608;
constexpr u32 RED_L = 75776;
constexpr u32 KV_L = 92160, QL_L = 101376, WS_L = 110592, WDS_L = 154624;
constexpr u32 WM_L = 134144, SV_L = 150528;
constexpr u32 SIG_L = 142336;
constexpr u32 X1L_L = 144384, GB_L = 152576;
constexpr u32 MISC_L = 162816, LDS_BYTES = 163840;
constexpr u32 SPIN_CAP = 1u << 17;

struct Params { const u16 *q, *k, *v, *sh, *sk, *Dv, *wg, *wu, *wd, *c, *wm; u16* out; char* ws; };

typedef __amdgpu_buffer_rsrc_t rsrc_t;
DI rsrc_t mkrs(const void* p, u32 bytes) { return __builtin_amdgcn_make_buffer_rsrc((void*)p, 0, bytes, 0x00020000); }
DI v4u mksrd(const void* p, u32 bytes) { v4u r = {(u32)(size_t)p, (u32)((size_t)p >> 32), bytes, 0x00020000u}; return r; }
DI v4u ld16(rsrc_t r, u32 vo, u32 so = 0) { return __builtin_amdgcn_raw_buffer_load_b128(r, vo, so, 0); }
DI v2u ld8c(rsrc_t r, u32 vo, u32 so = 0) { return __builtin_amdgcn_raw_buffer_load_b64(r, vo, so, 16); }
DI void st16c(v4u v, rsrc_t r, u32 vo, u32 so = 0) { __builtin_amdgcn_raw_buffer_store_b128(v, r, vo, so, 16); asm volatile("s_nop 1" ::"v"(v) : "memory"); }
DI void st16(v4u v, rsrc_t r, u32 vo, u32 so = 0) { __builtin_amdgcn_raw_buffer_store_b128(v, r, vo, so, 0); asm volatile("s_nop 1" ::"v"(v) : "memory"); }
DI float lo16(u32 x) { return __builtin_bit_cast(float, x << 16); }
DI float hi16(u32 x) { return __builtin_bit_cast(float, x & 0xffff0000u); }
DI v2f unpk(u32 x) { v2f r = {lo16(x), hi16(x)}; return r; }
DI u32 pk(v2f v) { return __builtin_bit_cast(u32, __builtin_convertvector(v, v2bf)); }
DI u32 pk(float a, float b) { v2f v = {a, b}; return pk(v); }
DI float rbf(float a) { return lo16(pk(a, a)); }
DI v4s trr(u32 a) { return __builtin_amdgcn_ds_read_tr16_b64_v4i16((LAS v4s*)a); }
DI v4u cat(v4s a, v4s b) { return __builtin_bit_cast(v4u, __builtin_shufflevector(a, b, 0, 1, 2, 3, 4, 5, 6, 7)); }
DI v4u lds16(u32 a) { return *(LAS v4u*)a; }
DI void sts16(u32 a, v4u v) { *(LAS v4u*)a = v; }
template <int C> DI float dppadd(float x) { return x + __builtin_bit_cast(float, __builtin_amdgcn_update_dpp(0, __builtin_bit_cast(int, x), C, 0xf, 0xf, true)); }
#define DRAIN() asm volatile("s_waitcnt vmcnt(0)" ::: "memory")
#define WAITV(n) asm volatile("s_waitcnt vmcnt(%0)" ::"i"(n) : "memory")
#define BAR() do { SB(); asm volatile("s_waitcnt lgkmcnt(0)\n\ts_barrier" ::: "memory"); SB(); } while (0)
#define KEEP8(a, o) asm volatile("" ::"v"(a[o]), "v"(a[o + 1]), "v"(a[o + 2]), "v"(a[o + 3]), "v"(a[o + 4]), "v"(a[o + 5]), "v"(a[o + 6]), "v"(a[o + 7]))
#define PAGE(n) asm volatile("s_branch 1f\n\t.p2alignl 12, 0xBF800000\n.Lwarm_" #n ":\n\ts_setpc_b64 vcc\n1:" ::: "memory")
#define ENTRY(n) asm volatile("s_branch 1f\n.Lwarm_" #n ":\n\ts_setpc_b64 vcc\n1:" ::: "memory")
#define WARM_IF(k, n) "s_cmp_lg_u32 %0, " #k "\n\ts_cbranch_scc1 1f\n\ts_call_b64 vcc, .Lwarm_" #n "\n1:\n\t"
#define WARM4(a, b, c, d) asm volatile(WARM_IF(0, a) WARM_IF(1, b) WARM_IF(2, c) WARM_IF(3, d)::"s"(w) : "vcc", "scc", "memory")
#define ORDER() __builtin_amdgcn_fence(__ATOMIC_ACQUIRE, "wavefront")
DI void glds(v4u srd, u32 vo, u32 so, u32 ldst) { asm volatile("s_mov_b32 m0, %0\n\ts_nop 0\n\tbuffer_load_dwordx4 %1, %2, %3 offen lds" ::"s"(ldst), "v"(vo), "s"(srd), "s"(so) : "memory"); }
DI void gldsc(v4u srd, u32 vo, u32 so, u32 ldst) { asm volatile("s_mov_b32 m0, %0\n\ts_nop 0\n\tbuffer_load_dwordx4 %1, %2, %3 offen sc1 lds" ::"s"(ldst), "v"(vo), "s"(srd), "s"(so) : "memory"); }
template <int N, int IMM> DI void lda(v4u srd, u32 vo, u32 so) { asm volatile("buffer_load_dwordx4 a[%c3:%c4], %0, %1, %2 offen offset:%c5 sc1" ::"v"(vo), "s"(srd), "s"(so), "i"(4 * N), "i"(4 * N + 3), "i"(IMM) : "memory"); }
template <int N, bool Z> DI void mfma32a(v16f& acc, v4u b) {
  if constexpr (Z) asm volatile("v_mfma_f32_32x32x16_bf16 %0, a[%c2:%c3], %1, 0" : "=&v"(acc) : "v"(b), "i"(4 * N), "i"(4 * N + 3));
  else asm volatile("v_mfma_f32_32x32x16_bf16 %0, a[%c2:%c3], %1, %0" : "+v"(acc) : "v"(b), "i"(4 * N), "i"(4 * N + 3));
}
template <bool Z> DI void mfma32(v16f& acc, v4u a, v4u b) {
  if constexpr (Z) asm volatile("v_mfma_f32_32x32x16_bf16 %0, %1, %2, 0" : "=&v"(acc) : "v"(a), "v"(b));
  else asm volatile("v_mfma_f32_32x32x16_bf16 %0, %1, %2, %0" : "+v"(acc) : "v"(a), "v"(b));
}
DI float dot2(u32 a, u32 b, float c) { return __builtin_amdgcn_fdot2_f32_bf16(__builtin_bit_cast(v2bf, a), __builtin_bit_cast(v2bf, b), c, false); }
DI float f_(u32 x) { return __builtin_bit_cast(float, x); }
DI u32 u_(float x) { return __builtin_bit_cast(u32, x); }
DI float fold32(float a, float b) { auto r = __builtin_amdgcn_permlane32_swap(u_(a), u_(b), false, false); return f_(r[0]) + f_(r[1]); }
DI float fold16(float a, float b) { auto r = __builtin_amdgcn_permlane16_swap(u_(a), u_(b), false, false); return f_(r[0]) + f_(r[1]); }
template <bool Z> DI void mfma16(v4f& acc, v4u a, v4u b) {
  if constexpr (Z) asm volatile("v_mfma_f32_16x16x32_bf16 %0, %1, %2, 0" : "=&v"(acc) : "v"(a), "v"(b));
  else asm volatile("v_mfma_f32_16x16x32_bf16 %0, %1, %2, %0" : "+v"(acc) : "v"(a), "v"(b));
}
template <int I, int N, class Fn> DI void sfor(Fn&& f) { if constexpr (I < N) { f(std::integral_constant<int, I>{}); sfor<I + 1, N>(f); } }
DI void poll(const char* p, u64 want) {
#pragma unroll 1
  for (u32 sp = 0; sp < SPIN_CAP; ++sp) { if (__all(__hip_atomic_load((GAS u64*)p, RLX_AGENT) == want)) break; __builtin_amdgcn_s_sleep(1); }
  ORDER();
}
template <int N> DI void waitbar() { asm volatile("s_waitcnt vmcnt(%0)\n\ts_barrier" ::"i"(N) : "memory"); }
DI u32 opq(u32 x) { asm("" : "+v"(x)); return x; }
template <int IMM> DI void ald16o(v4u& d, v4u srd, u32 vo, u32 so) { asm volatile("buffer_load_dwordx4 %0, %1, %2, %3 offen offset:%c4 sc1 ; AL" : "=v"(d) : "v"(vo), "s"(srd), "s"(so), "i"(IMM) : "memory"); }
DI void ald8(v2u& d, v4u srd, u32 vo, u32 so) { asm volatile("buffer_load_dwordx2 %0, %1, %2, %3 offen sc1 ; AL" : "=v"(d) : "v"(vo), "s"(srd), "s"(so) : "memory"); }
DI void ald4(u32& d, v4u srd, u32 vo, u32 so) { asm volatile("buffer_load_dword %0, %1, %2, %3 offen sc1 ; AL" : "=v"(d) : "v"(vo), "s"(srd), "s"(so) : "memory"); }
template <class T> DI void pin(T& a) { asm volatile("; PIN %0" : "+v"(a)::"memory"); }
template <int N, bool Z> DI void mfma32b(v16f& acc, v4u a) {
  if constexpr (Z) asm volatile("v_mfma_f32_32x32x16_bf16 %0, %1, a[%c2:%c3], 0" : "=&v"(acc) : "v"(a), "i"(4 * N), "i"(4 * N + 3));
  else asm volatile("v_mfma_f32_32x32x16_bf16 %0, %1, a[%c2:%c3], %0" : "+v"(acc) : "v"(a), "i"(4 * N), "i"(4 * N + 3));
}
DI u64 rtag(u64 tag) { return tag & 0xBFFFBFFFBFFFBFFFull; }
DI bool fresh(v4u r, u64 rt) { return r.y == (u32)rt && r.w == (u32)(rt >> 32); }
DI u32 stale(v4u r, u64 rt, u32 a) { return a | (r.y ^ (u32)rt) | (r.w ^ (u32)(rt >> 32)); }
DI v4u reread(rsrc_t rs, u32 vo, u32 so, u64 tag) { v4u r;
#pragma unroll 1
  for (u32 sp = 0; sp < (1u << 17); ++sp) { r = __builtin_amdgcn_raw_buffer_load_b128(rs, vo, so, 16); if (__all(fresh(r, tag))) break; __builtin_amdgcn_s_sleep(1); }
  return r; }
DI u32 lds_inc(u32 a) { return __hip_atomic_fetch_add((LAS u32*)a, 1u, __ATOMIC_RELAXED, __HIP_MEMORY_SCOPE_WORKGROUP); }
DI void add64(v4u srd, u32 vo, u32 so) { const v2u one = {1, 0}; asm volatile("buffer_atomic_add_x2 %0, %1, %2, %3 offen sc1" ::"v"(one), "v"(vo), "s"(srd), "s"(so) : "memory"); }
DI u32 eqmask(u32 a, u32 b) { u32 m; asm("s_cmp_eq_u32 %1, %2\n\ts_cselect_b32 %0, -1, 0" : "=s"(m) : "s"(a), "i"(b) : "scc"); return m; }
DI void ald2(u32& d, v4u srd, u32 so) { asm volatile("buffer_load_ushort %0, off, %1, %2 ; AL" : "=v"(d) : "s"(srd), "s"(so) : "memory"); }
template <int E> DI u32 mix1(v4u qv, v4u uw, const float (&yy)[8], const v4u (&st)[4], float sa, float a1, float ga, float dv) {
  const v2f x = unpk(qv[E]), u = {hi16(uw[3 - E]), lo16(uw[3 - E])}, yv = {yy[2 * E], yy[2 * E + 1]};
  const v4f n4 = __builtin_bit_cast(v4f, st[E >> 1]), r4 = __builtin_bit_cast(v4f, st[2 + (E >> 1)]);
  const v2f nm = {n4[2 * (E & 1)], n4[2 * (E & 1) + 1]}, rs = {r4[2 * (E & 1)], r4[2 * (E & 1) + 1]};
  return pk(x + ga * (((x * rs + nm) * a1 + sa) * (yv + u * dv)));
}
template <int E> DI v4u quad(const v16f& a) { return __builtin_bit_cast(v4u, v4f{a[E], a[E + 1], a[E + 2], a[E + 3]}); }
template <class T> DI void pins(T& a) { asm volatile("" : "+s"(a)); }
template <int L, int G> DI void gldsk(v4u srd, u32 vo, u32 gb, u32 lb) { u32 so;
  asm volatile("s_add_u32 m0, %4, %c5\n\ts_add_u32 %0, %3, %c6\n\tbuffer_load_dwordx4 %1, %2, %0 offen sc1 lds" : "=&s"(so) : "v"(vo), "s"(srd), "s"(gb), "s"(lb), "i"(L), "i"(G) : "scc", "memory"); }
DI void gu1(float& m0, float& m1, float a, float b, float c, float d, float r) { asm volatile("v_mul_f32_e32 %0, %2, %3\n\tv_mul_f32_e32 %1, %4, %5\n\tv_mul_f32_e32 %0, %0, %6" : "=&v"(m0), "=&v"(m1) : "v"(a), "v"(b), "v"(c), "v"(d), "v"(r)); }
DI u32 gu2(float m0, float m1, float r) { u32 o; asm volatile("v_mul_f32_e32 %0, %2, %3\n\tv_cvt_pk_bf16_f32 %0, %1, %0" : "=&v"(o) : "v"(m0), "v"(m1), "v"(r)); return o; }
DI float sum4(float a, float b, float c, float d) { float r, u; asm volatile("v_add_f32_e32 %0, %2, %3\n\tv_add_f32_e32 %1, %4, %5\n\tv_add_f32_e32 %0, %0, %1" : "=&v"(r), "=&v"(u) : "v"(a), "v"(b), "v"(c), "v"(d)); return r; }
template <int HI> DI float axpy(float a, float x, u32 y) { float r;
  if constexpr (HI) asm volatile("v_and_b32_e32 %0, 0xffff0000, %3\n\tv_fmac_f32_e32 %0, %1, %2" : "=&v"(r) : "v"(a), "v"(x), "v"(y));
  else asm volatile("v_lshlrev_b32_e32 %0, 16, %3\n\tv_fmac_f32_e32 %0, %1, %2" : "=&v"(r) : "v"(a), "v"(x), "v"(y));
  return r; }
DI void nops(v16f& a) { asm volatile("s_nop 7\n\ts_nop 1" : "+v"(a)::"memory"); }
DI void pin2(u32& a, u32& b) { asm volatile("" : "+v"(a), "+v"(b)); }
DI void peek(u64& fl, const char* p) { asm volatile("global_load_dwordx2 %0, %1, off sc1 ; AL" : "=v"(fl) : "v"(p) : "memory"); }
template <int N> DI void check(u64& fl, const char* p, u64 want) { u32 n = 1u << 17;
  asm volatile("s_waitcnt vmcnt(%4) ; PIN %0\n"
               "1:\n\tv_cmp_ne_u64 vcc, %3, %0\n\ts_nop 4\n\ts_cbranch_vccz 2f\n\ts_sub_u32 %1, %1, 1\n\ts_cbranch_scc1 2f\n\t"
               "s_sleep 1\n\tglobal_load_dwordx2 %0, %2, off sc1\n\ts_waitcnt vmcnt(0)\n\ts_branch 1b\n2:"
               : "+v"(fl), "+s"(n) : "v"(p), "s"(want), "i"(N) : "vcc", "scc", "memory"); }
__global__ __launch_bounds__(256, 1) void mega(Params P) {
  extern __shared__ __attribute__((aligned(16))) char smem_[];
  asm volatile("" ::: "a0", "a255");
  const int b = blockIdx.x, t = threadIdx.x, lane = t & 63, w = __builtin_amdgcn_readfirstlane(t >> 6);
  const int j = b & 7, i = b >> 3;
  u64 pc; u32 code_len;
  asm volatile("s_getpc_b64 %0\n.Lmega_begin:\n\ts_mov_b32 %1, .Lmega_end-.Lmega_begin\n\ts_max_u32 %1, %1, .Lmega_end2-.Lmega_begin" : "=s"(pc), "=s"(code_len)::"scc");
  const int cp = t & 31, rg = t >> 5;
  u32 kk[11], vv[8], qq[8]; v4u sh;
  {
    const rsrc_t rk = mkrs(P.k, S * D * 2), rv = mkrs(P.v, S * D * 2), rs = mkrs(P.sh, D * 8);
    const u32 o = (64 * i + 8 * rg) * 1024 + 128 * j + 4 * cp, om = o - 3072, op = o + 4096;
#pragma unroll
    for (int m = 0; m < 3; ++m) kk[m] = __builtin_amdgcn_raw_buffer_load_b32(rk, om + m * 1024, 0, 0);
#pragma unroll
    for (int m = 0; m < 8; ++m) kk[3 + m] = __builtin_amdgcn_raw_buffer_load_b32(rk, (m < 4 ? o : op) + (m & 3) * 1024, 0, 0);
#pragma unroll
    for (int m = 0; m < 8; ++m) vv[m] = __builtin_amdgcn_raw_buffer_load_b32(rv, (m < 4 ? o : op) + (m & 3) * 1024, 0, 0);
    sh = ld16(rs, cp * 16, j * 512);
    { SB(); const rsrc_t rq = mkrs(P.q, S * D * 2);
#pragma unroll
      for (int m = 0; m < 8; ++m) qq[m] = __builtin_amdgcn_raw_buffer_load_b32(rq, (m < 4 ? o : op) + (m & 3) * 1024, 0, 0); }
  }
  const u32 pf0 = __builtin_amdgcn_raw_buffer_load_b32(mkrs((const void*)(pc & ~127ull), code_len), t < 48 ? ((i & 7) * 48 + t) * 128 : 0x80000000u, 0, 0);
  SB();
  const rsrc_t rws = mkrs(P.ws, WS_BYTES);
  const v4u sws = mksrd(P.ws, WS_BYTES);
  const int g = lane >> 4, i16 = lane & 15, q4 = i16 >> 2, p4 = lane & 3, h = lane >> 5;
  const bool is_stat = i >= 28;
  const int mt = 4 * (j >> 1) + (i & 3), nt = 7 * (j & 1) + (i >> 2);
  const int zs = 4 * j + (i - 28), su = (zs >> 1) * 4 + w;
  const int mt2 = 4 * j + (i & 3), dt = i >> 2;
  const u32 l16 = lane * 16, sm_ = eqmask(i >> 2, 7); u32 wso = WG_OFF + ((((su < 56 ? su : 0) * 65536 + (zs & 1) * 32768) & sm_) | ((nt * 4 + w) * 65536 & ~sm_)), wdso = WD_OFF + (dt * 112 + 28 * w) * 2048;
  const u32 sw64 = (lane >> 3) * 128 + (((lane & 7) ^ ((g & 1) << 2)) << 4);
  const u32 sw64g = (lane >> 3) * RS + (((lane & 7) ^ ((g & 1) << 2)) << 4);
  const u32 tr64 = (8 * h + q4) * 128 + ((q4 >> 1) << 6) + (g & 1) * 32 + p4 * 8;
  u64 tag = 0;
  auto cnt_addr = [&](u32 c) { return P.ws + c + (j * 16 + i16) * FS; };
  auto flag = [&](u32 off, bool on = true) { __builtin_amdgcn_raw_buffer_store_b64(__builtin_bit_cast(v2u, tag), rws, lane || !on ? 0x80000000u : 0, off, 16); asm volatile("" ::: "memory"); };

  const u32 shard = (lane * 16 + 2 * j + (i & 1)) * FS;

  const int ch = w >> 1, kh = w & 1, d = 64 * j + 2 * i + ch;
  u32 aA = KC_L + ch * KC_CH + (i16 & 7) * KC_CS + ((i16 >> 3) + g) * 16 + kh * 256, aB = WL_L + ch * WL_CH + 480 - 32 * i16 + 16 * g + kh * 256;
  u32 s2 = (256 * (4 * kh + (g & 1)) + 16 * i16 + 8 * (g >> 1)) * 2, uwa = WL_L + ch * WL_CH + 3056 - s2, rdw = RED_L + l16 + w * 4096, rdr = RED_L + l16 + (w ^ 1) * 4096;
  u32 wdo = WD_OFF + (j * 112 + 4 * i + w) * 2048, qso = QT_OFF + d * RS, xso = X1_OFF + d * RS, mso = MODP_OFF + d * 4, gso = MODP_OFF + (5 * 512 + 64 * dt) * 4;
  u32 mvo = (p4 * 3072 + (q4 == 3 ? 4 : q4) * 512) * 4, gvo = (p4 * 3072 + (t >> 2)) * 4, gbw = p4 ? MISC_L + t * 4 : GB_L + (t >> 2) * 4;
  u32 xda = WDS_L + w * 2048 + (4 * h + q4) * 128 + ((q4 >> 1) << 6) + (g & 1) * 32 + p4 * 8;
  const char* pc0 = cnt_addr(C0);

  const bool do_mod = b < 192, do_w = b >= 32;
  const int cg = b % 48, ksp = b / 48, wn = (b - 32) >> 4, kb = b & 15;
  v4u sk0, sk1, sk2, sk3, sa_[16]; u32 cr[2], draw;
  auto side_loads = [&](int part) {
    const u32 m1 = eqmask(w, 1), m2 = eqmask(w, 2), m3 = eqmask(w, 3);
    auto sel = [&](u32 x1, u32 x2, u32 x3) { return (x1 & m1) | (x2 & m2) | (x3 & m3); };
    auto selp = [&](const u16* x1, const u16* x2, const u16* x3) { const u64 n2 = (u64)m2 << 32 | m2, n3 = (u64)m3 << 32 | m3; return (const void*)(((size_t)x1 & ~(n2 | n3)) | ((size_t)x2 & n2) | ((size_t)x3 & n3)); };
    const u32 by = sel(S * D * 2, do_mod ? D * 6 * D * 2 : 0, do_w ? D * F * 2 : 0);
    const u32 vb = (lane >> (3 + (m3 & 1))) * sel(1024, 6144, 3584) + (lane & sel(7, 7, 15)) * 16, va = (lane * 16 & m1) | (vb & ~m1);
    const u32 oa = sel(8192 * b, 128 * ksp * 6144 + 128 * cg, do_w ? 32 * kb * 3584 + 256 * wn : 0), ob = sel(0, oa + 64 * 6144, oa);
    const u32 ta = sel(1024, 8 * 6144, 4 * 3584);
    const rsrc_t ra = mkrs(selp(P.q, P.wm, P.wg), by), rb = mkrs(selp(P.q, P.wm, P.wu), by & ~m1), rc = mkrs(P.c, D * 2);
    if (part & 1) { cr[0] = __builtin_amdgcn_raw_buffer_load_b16(rc, (128 * ksp + lane) * 2, 0, 0); cr[1] = __builtin_amdgcn_raw_buffer_load_b16(rc, (128 * ksp + 64 + lane) * 2, 0, 0);
#pragma unroll
      for (int n = 0; n < 8; ++n) sa_[n] = ld16(ra, va, oa + n * ta); }
    if (part & 2) {
#pragma unroll
      for (int n = 0; n < 8; ++n) sa_[8 + n] = ld16(rb, vb, ob + n * ta); }
  };
  { v2u ep; const rsrc_t rf = mkrs(P.sk, D * S * 2);
    ep = ld8c(rws, 0, EPOCH_OFF);
    SB();
    const int tt = t & 127; const u32 of = (64 * j + 2 * i + (t >> 7)) * 4096 + tt * 16;
    sk0 = ld16(rf, tt ? of - 16 : 0x80000000u); sk1 = ld16(rf, of); sk2 = ld16(rf, of + 2032); sk3 = ld16(rf, of + 2048);
    SB();
    side_loads(1);
    SB();
    const v2f t0 = {lo16(sh.x), lo16(sh.z)}, t1 = {hi16(sh.x), hi16(sh.z)}, t2 = {lo16(sh.y), lo16(sh.w)}, t3 = {hi16(sh.y), hi16(sh.w)};
    v2f kf[11], y[8];
#pragma unroll
    for (int m = 0; m < 11; ++m) kf[m] = unpk(kk[m]);
#pragma unroll
    for (int m = 0; m < 8; ++m) y[m] = (kf[m] * t3 + kf[m + 1] * t2 + kf[m + 2] * t1 + kf[m + 3] * t0) * unpk(vv[m]);
    const u32 ad = KV_L + cp * 288 + (7 - rg) * 16;
    sts16(ad, v4u{pk(y[7].x, y[6].x), pk(y[5].x, y[4].x), pk(y[3].x, y[2].x), pk(y[1].x, y[0].x)});
    sts16(ad + 144, v4u{pk(y[7].y, y[6].y), pk(y[5].y, y[4].y), pk(y[3].y, y[2].y), pk(y[1].y, y[0].y)});
    { const u32 aq = QL_L + cp * 288 + rg * 16;
      sts16(aq, v4u{__builtin_amdgcn_perm(qq[1], qq[0], 0x05040100), __builtin_amdgcn_perm(qq[3], qq[2], 0x05040100), __builtin_amdgcn_perm(qq[5], qq[4], 0x05040100), __builtin_amdgcn_perm(qq[7], qq[6], 0x05040100)});
      sts16(aq + 144, v4u{__builtin_amdgcn_perm(qq[1], qq[0], 0x07060302), __builtin_amdgcn_perm(qq[3], qq[2], 0x07060302), __builtin_amdgcn_perm(qq[5], qq[4], 0x07060302), __builtin_amdgcn_perm(qq[7], qq[6], 0x07060302)}); }
    SB();
    side_loads(2);
    { const v4u swd = mksrd(P.wd, is_stat ? 0 : F * D * 2); const u32 wdv = (lane >> 3) * 1024 + (sw64 & 127), wds = (64 * i + 16 * w) * 1024 + 128 * j;
      ald2(draw, mksrd(P.Dv, D * 2), d * 2);
      glds(swd, wdv, wds, WDS_L + w * 2048); glds(swd, wdv, wds + 8192, WDS_L + w * 2048 + 1024); }
    SB();
    tag = (((u64)(u32)__builtin_amdgcn_readfirstlane(ep.y) << 32) | (u32)__builtin_amdgcn_readfirstlane(ep.x)) + 1;
    asm volatile("" ::"v"(pf0));
    *(LAS u32*)(MISC_L + t * 4) = 0; }
  BAR();
  auto front = [&](auto rolec) { constexpr int R = rolec;
    auto build_kc = [&]() {
      const u32 base = opq(KC_L + (t >> 7) * KC_CH + (1 + (t & 127)) * 16);
      auto one = [&](v4u a, v4u c, u32 ad) {
        u32 d[8] = {a.x, a.y, a.z, a.w, c.x, c.y, c.z, c.w}, s[7];
#pragma unroll
        for (int m = 0; m < 7; ++m) s[m] = __builtin_amdgcn_alignbit(d[m + 1], d[m], 16);
#pragma unroll
        for (int r = 0; r < 8; ++r) { v4u o; if (r & 1) { const int m = (r + 1) / 2; o = v4u{d[m], d[m + 1], d[m + 2], d[m + 3]}; } else { const int m = r / 2; o = v4u{s[m], s[m + 1], s[m + 2], s[m + 3]}; }
          sts16(ad + r * KC_CS, o); }
      };
      SB(); one(sk0, sk1, base); one(sk2, sk3, base + 2048);
      const v4u z = {0, 0, 0, 0};
      if constexpr (R == 0) if (lane < 16) sts16(KC_L + (lane >> 3) * KC_CH + (lane & 7) * KC_CS, z);
      if constexpr (R == 1) if ((lane & 31) < 30) sts16(WL_L + h * WL_CH + 4096 + (lane & 31) * 16, z);
      SB();
    };
    if constexpr (R == 0) {
      v4u x[8];
#pragma unroll
      for (int n = 0; n < 8; ++n) x[n] = lds16(opq(KV_L + (lane >> 3) * 144 + (lane & 7) * 16) + n * 1152);
      SB();
#pragma unroll
      for (int n = 0; n < 8; ++n) st16c(x[n], rws, (lane >> 3) * RS + (lane & 7) * 16, WT_OFF + (64 * j + 8 * n) * RS + (1984 - 64 * i) * 2);
      __builtin_amdgcn_raw_buffer_store_b64(__builtin_bit_cast(v2u, tag << 8), rws, (lane < 16 ? CNT_OFF + (lane * 16 + 2 * j + i) * FS : 0x80000000u) | (~eqmask(i >> 1, 0) & 0x80000000u), 0, 16);
      DRAIN();
      flag(FLAGA_OFF + (j * 32 + i) * FS);
      KEEP8(sa_, 0); KEEP8(sa_, 8); asm volatile("" ::"v"(cr[0]), "v"(cr[1]));
      build_kc();
    } else {
      if constexpr (R == 1) {
        { v4u x[8];
#pragma unroll
          for (int n = 0; n < 8; ++n) x[n] = lds16(opq(QL_L + (lane >> 3) * 144 + (lane & 7) * 16) + n * 1152);
          SB();
#pragma unroll
          for (int n = 0; n < 8; ++n) st16c(x[n], rws, (lane >> 3) * RS + (lane & 7) * 16, QT_OFF + (64 * j + 8 * n) * RS + 128 * i);
          SB(); }
          float sm[8], sq[8];
#pragma unroll
        for (int r = 0; r < 8; ++r) { sm[r] = 0; sq[r] = 0;
#pragma unroll
          for (int e = 0; e < 4; ++e) { sm[r] = dot2(sa_[r][e], 0x3f803f80u, sm[r]); sq[r] = dot2(sa_[r][e], sa_[r][e], sq[r]); } }
        const float a0 = fold16(fold32(sm[0], sm[4]), fold32(sm[2], sm[6])), a1 = fold16(fold32(sm[1], sm[5]), fold32(sm[3], sm[7]));
        const float b0 = fold16(fold32(sq[0], sq[4]), fold32(sq[2], sq[6])), b1 = fold16(fold32(sq[1], sq[5]), fold32(sq[3], sq[7]));
        float c0 = dppadd<0x128>(a0), c1 = dppadd<0x128>(a1), d0 = dppadd<0x128>(b0), d1 = dppadd<0x128>(b1);
        asm volatile("" : "+v"(c0), "+v"(c1), "+v"(d0), "+v"(d1));
        const bool od = lane & 8;
        float m = dppadd<0x141>(dppadd<0x4E>(dppadd<0xB1>(od ? c1 : c0))) * (1.f / D), v = dppadd<0x141>(dppadd<0x4E>(dppadd<0xB1>(od ? d1 : d0))) * (1.f / D);
        asm volatile("" : "+v"(m), "+v"(v));
        const float rs = 1.f / rbf(sqrtf(rbf(rbf(fmaxf(v - m * m, 0.f)) + EPS)));
        float rv = (lane & 1) ? rs : -rbf(m) * rs; pin(rv);
        SB();
        __builtin_amdgcn_raw_buffer_store_b32(u_(rv), rws, (lane & 6) ? 0x80000000u : ((lane & 1) * 2 + h) * 4096 + ((lane >> 3) & 3) * 4, ST1_OFF + b * 16, 16);
      }
      if constexpr (R == 2) {
#pragma unroll
        for (int n = 0; n < 2; ++n) { const float x = lo16(cr[n]); *(LAS u16*)(SV_L + n * 128 + lane * 2) = (u16)pk(x / (1.f + __expf(-x)), 0.f); }
#pragma unroll
        for (int n = 0; n < 16; ++n) sts16(opq(WM_L + sw64) + n * 1024, sa_[n]);
          v4u fa[8]; v4s fw[8][2][2]; v16f ma[2];
#pragma unroll
        for (int ks = 0; ks < 8; ++ks) { fa[ks] = lds16(opq(SV_L + h * 16) + ks * 32);
#pragma unroll
          for (int ct = 0; ct < 2; ++ct)
#pragma unroll
            for (int tt = 0; tt < 2; ++tt) fw[ks][ct][tt] = trr(opq(WM_L + (tr64 ^ (ct << 6))) + ks * 2048 + tt * 512); }
        SB();
        sfor<0, 16>([&](auto c) { constexpr int ks = c >> 1, ct = c & 1; mfma32<ks == 0>(ma[ct], fa[ks], cat(fw[ks][ct][0], fw[ks][ct][1])); });
        asm volatile("s_nop 7\n\ts_nop 7" : "+v"(ma[0]), "+v"(ma[1])::"memory");
        const float m0 = ma[0][0], m1 = ma[1][0];
        SB();
        __builtin_amdgcn_raw_buffer_store_b32(u_(h ? m1 : m0), rws, do_mod ? lane * 4 : 0x80000000u, MODP_OFF + (ksp * 3072 + 64 * cg) * 4, 16);
      }
      if constexpr (R == 3) { const int mat = g & 1, hh = g >> 1;
        {
#pragma unroll
          for (int n = 0; n < 16; ++n) sts16(opq(WS_L + g * 256 + ((((i16 >> 1) ^ g ^ ((n >> 3) << 2)) << 5) | ((i16 & 1) << 4))) + n * 1024, sa_[n]);
          v4s x[8][2][2];
#pragma unroll
          for (int ft = 0; ft < 8; ++ft)
#pragma unroll
            for (int ks = 0; ks < 2; ++ks)
#pragma unroll
              for (int tt = 0; tt < 2; ++tt) x[ft][ks][tt] = trr(opq(WS_L + mat * 8192 + (8 * hh + q4) * 256 + ((ft ^ (q4 | (mat << 2))) << 5) + p4 * 8) + ks * 4096 + tt * 1024);
          SB();
          const u32 wv = do_w ? l16 : 0x80000000u;
#pragma unroll
          for (int ft = 0; ft < 8; ++ft)
#pragma unroll
            for (int ks = 0; ks < 2; ++ks) st16c(cat(x[ft][ks][0], x[ft][ks][1]), rws, wv + ks * 2048 + (ft & 1) * 1024, WG_OFF + ((do_w ? wn : 0) * 4 + (ft >> 1)) * 65536 + kb * 4096); }
      }
      const char* ini = P.ws + FLAGA_OFF + (j * 32 + (i & 1)) * FS;
      const u64 f0 = __hip_atomic_load((GAS u64*)ini, RLX_AGENT);
      DRAIN();
      if constexpr (R == 1) KEEP8(sa_, 8);
      if constexpr (R != 2) asm volatile("" ::"v"(cr[0]), "v"(cr[1]));
      if (!__all(f0 == tag)) poll(ini, tag);
      add64(sws, __builtin_amdgcn_readfirstlane(lds_inc(MISC_L + lane * 4)) == 2 && lane < 8 ? shard : 0x80000000u, C0);
      build_kc();
    }
  };
  if (w == 0) front(std::integral_constant<int, 0>{}); else if (w == 1) front(std::integral_constant<int, 1>{});
  else if (w == 2) front(std::integral_constant<int, 2>{}); else front(std::integral_constant<int, 3>{});
  SB();
  WARM4(conv, st, epi, conv);
  pins(wso); pins(wdo); pins(qso); pins(xso); pins(mso); pins(gso); pin(aA); pin(aB); pin(s2); pin(mvo); pin(gvo); pin(pc0); pin(rdw); pin(rdr); pin(uwa); pin(xda); pin(gbw);
  SB();
  if (w == 0) {
      poll(P.ws + FLAGA_OFF + (j * 32 + (lane & 31)) * FS, tag);
#pragma unroll
      for (int n = 0; n < 8; ++n) gldsc(sws, lane * 16, WT_OFF + (64 * j + 2 * i + (n >> 2)) * RS + (n & 3) * 1024, WL_L + (n >> 2) * WL_CH + (n & 3) * 1024);
  }
  u64 fl;
  if (w == 0) DRAIN();
  BAR();
  PAGE(conv);

  const u64 full = (tag << 8) + 16, rt = rtag(tag);

  v2u mp[8];
  {
    v4u fc[2][16], qv[2], st[2][4]; v4f acc[8], y[4]; u32 mraw, graw; float gq, mq, sa, a1, ga, sg, dv;
    fc[0][3] = lds16(aA + 1536);
    fc[0][11] = lds16(aB + 2048);
    fc[0][0] = lds16(aA + 0);
    fc[0][9] = lds16(aB + 3072);
    fc[0][12] = lds16(aB + 1536);
    fc[0][1] = lds16(aA + 512);
    fc[0][10] = lds16(aB + 2560);
    fc[0][2] = lds16(aA + 1024);
    fc[0][8] = lds16(aB + 3584);
    fc[0][4] = lds16(aA + 2048);
    fc[0][13] = lds16(aB + 1024);
    fc[0][14] = lds16(aB + 512);
    fc[0][5] = lds16(aA + 2560);
    fc[0][6] = lds16(aA + 3072);
    fc[0][7] = lds16(aA + 3584);
    fc[0][15] = lds16(aB + 0);
    peek(fl, pc0);
    SB();
    v4s xd[2][2];
    mfma16<true>(acc[6], fc[0][3], fc[0][11]);
    fc[1][3] = lds16(aA + 1600);
    SB();
    mfma16<true>(acc[1], fc[0][0], fc[0][9]);
    SB();
    mfma16<true>(acc[7], fc[0][3], fc[0][12]);
    fc[1][11] = lds16(aB + 2112);
    SB();
    mfma16<true>(acc[3], fc[0][1], fc[0][10]);
    SB();
    mfma16<true>(acc[4], fc[0][2], fc[0][10]);
    fc[1][0] = lds16(aA + 64);
    SB();
    mfma16<true>(acc[2], fc[0][1], fc[0][9]);
    SB();
    mfma16<true>(acc[5], fc[0][3], fc[0][10]);
    fc[1][9] = lds16(aB + 3136);
    SB();
    mfma16<true>(acc[0], fc[0][0], fc[0][8]);
    SB();
    mfma16<false>(acc[7], fc[0][4], fc[0][11]);
    fc[1][12] = lds16(aB + 1600);
    SB();
    mfma16<false>(acc[6], fc[0][2], fc[0][12]);
    SB();
    mfma16<false>(acc[5], fc[0][2], fc[0][11]);
    fc[1][1] = lds16(aA + 576);
    SB();
    mfma16<false>(acc[3], fc[0][2], fc[0][9]);
    SB();
    mfma16<false>(acc[7], fc[0][2], fc[0][13]);
    fc[1][10] = lds16(aB + 2624);
    SB();
    mfma16<false>(acc[4], fc[0][1], fc[0][11]);
    SB();
    mfma16<false>(acc[5], fc[0][1], fc[0][12]);
    fc[1][2] = lds16(aA + 1088);
    SB();
    mfma16<false>(acc[1], fc[0][1], fc[0][8]);
    SB();
    mfma16<false>(acc[2], fc[0][2], fc[0][8]);
    fc[1][8] = lds16(aB + 3648);
    SB();
    mfma16<false>(acc[4], fc[0][0], fc[0][12]);
    SB();
    mfma16<false>(acc[6], fc[0][4], fc[0][10]);
    fc[1][4] = lds16(aA + 2112);
    SB();
    mfma16<false>(acc[7], fc[0][1], fc[0][14]);
    SB();
    mfma16<false>(acc[4], fc[0][3], fc[0][9]);
    fc[1][13] = lds16(aB + 1088);
    SB();
    mfma16<false>(acc[3], fc[0][3], fc[0][8]);
    SB();
    mfma16<false>(acc[2], fc[0][0], fc[0][10]);
    fc[1][14] = lds16(aB + 576);
    SB();
    mfma16<false>(acc[6], fc[0][1], fc[0][13]);
    SB();
    mfma16<false>(acc[3], fc[0][0], fc[0][11]);
    fc[1][5] = lds16(aA + 2624);
    SB();
    mfma16<false>(acc[7], fc[0][5], fc[0][10]);
    SB();
    mfma16<false>(acc[6], fc[0][6], fc[0][8]);
    fc[1][6] = lds16(aA + 3136);
    SB();
    mfma16<false>(acc[5], fc[0][4], fc[0][9]);
    SB();
    mfma16<false>(acc[7], fc[0][6], fc[0][9]);
    fc[1][7] = lds16(aA + 3648);
    SB();
    mfma16<false>(acc[4], fc[0][4], fc[0][8]);
    SB();
    mfma16<false>(acc[6], fc[0][5], fc[0][9]);
    fc[1][15] = lds16(aB + 64);
    SB();
    mfma16<false>(acc[5], fc[0][5], fc[0][8]);
    SB();
    mfma16<false>(acc[7], fc[0][7], fc[0][8]);
    SB();
    mfma16<false>(acc[6], fc[0][0], fc[0][14]);
    SB();
    mfma16<false>(acc[5], fc[0][0], fc[0][13]);
    SB();
    mfma16<false>(acc[7], fc[0][0], fc[0][15]);
    SB();
    check<0>(fl, pc0, full);
    if (is_stat) { _Pragma("unroll") for (int m = 0; m < 4; ++m) { ald8(mp[m], sws, t * 8, MODP_OFF + (m * 3072 + 4 * 512) * 4); ald8(mp[4 + m], sws, t * 8, MODP_OFF + (m * 3072 + 3 * 512) * 4); } }
    SB();
    mfma16<false>(acc[6], fc[1][3], fc[1][11]);
    fc[0][3] = lds16(aA + 1664);
    SB();
    mfma16<false>(acc[1], fc[1][0], fc[1][9]);
    ald4(mraw, sws, mvo, mso);
    SB();
    mfma16<false>(acc[7], fc[1][3], fc[1][12]);
    fc[0][11] = lds16(aB + 2176);
    SB();
    mfma16<false>(acc[3], fc[1][1], fc[1][10]);
    SB();
    mfma16<false>(acc[4], fc[1][2], fc[1][10]);
    fc[0][0] = lds16(aA + 128);
    SB();
    mfma16<false>(acc[2], fc[1][1], fc[1][9]);
    ald4(graw, sws, gvo, gso);
    SB();
    mfma16<false>(acc[5], fc[1][3], fc[1][10]);
    fc[0][9] = lds16(aB + 3200);
    SB();
    mfma16<false>(acc[0], fc[1][0], fc[1][8]);
    SB();
    mfma16<false>(acc[7], fc[1][4], fc[1][11]);
    fc[0][12] = lds16(aB + 1664);
    SB();
    mfma16<false>(acc[6], fc[1][2], fc[1][12]);
    ald16o<0>(st[0][0], sws, s2, ST1_OFF + 0);
    SB();
    mfma16<false>(acc[5], fc[1][2], fc[1][11]);
    fc[0][1] = lds16(aA + 640);
    SB();
    mfma16<false>(acc[3], fc[1][2], fc[1][9]);
    SB();
    mfma16<false>(acc[7], fc[1][2], fc[1][13]);
    fc[0][10] = lds16(aB + 2688);
    SB();
    mfma16<false>(acc[4], fc[1][1], fc[1][11]);
    SB();
    mfma16<false>(acc[5], fc[1][1], fc[1][12]);
    fc[0][2] = lds16(aA + 1152);
    ald16o<0>(st[0][1], sws, s2, ST1_OFF + 4096);
    SB();
    mfma16<false>(acc[1], fc[1][1], fc[1][8]);
    SB();
    mfma16<false>(acc[2], fc[1][2], fc[1][8]);
    fc[0][8] = lds16(aB + 3712);
    SB();
    mfma16<false>(acc[4], fc[1][0], fc[1][12]);
    SB();
    mfma16<false>(acc[6], fc[1][4], fc[1][10]);
    fc[0][4] = lds16(aA + 2176);
    ald16o<0>(st[0][2], sws, s2, ST1_OFF + 8192);
    SB();
    mfma16<false>(acc[7], fc[1][1], fc[1][14]);
    SB();
    mfma16<false>(acc[4], fc[1][3], fc[1][9]);
    fc[0][13] = lds16(aB + 1152);
    SB();
    mfma16<false>(acc[3], fc[1][3], fc[1][8]);
    SB();
    mfma16<false>(acc[2], fc[1][0], fc[1][10]);
    fc[0][14] = lds16(aB + 640);
    SB();
    mfma16<false>(acc[6], fc[1][1], fc[1][13]);
    ald16o<0>(st[0][3], sws, s2, ST1_OFF + 12288);
    SB();
    mfma16<false>(acc[3], fc[1][0], fc[1][11]);
    fc[0][5] = lds16(aA + 2688);
    SB();
    mfma16<false>(acc[7], fc[1][5], fc[1][10]);
    SB();
    mfma16<false>(acc[6], fc[1][6], fc[1][8]);
    fc[0][6] = lds16(aA + 3200);
    SB();
    mfma16<false>(acc[5], fc[1][4], fc[1][9]);
    ald16o<0>(qv[0], sws, s2, qso);
    SB();
    mfma16<false>(acc[7], fc[1][6], fc[1][9]);
    fc[0][7] = lds16(aA + 3712);
    SB();
    mfma16<false>(acc[4], fc[1][4], fc[1][8]);
    SB();
    mfma16<false>(acc[6], fc[1][5], fc[1][9]);
    fc[0][15] = lds16(aB + 128);
    SB();
    mfma16<false>(acc[5], fc[1][5], fc[1][8]);
    ald16o<1024>(st[1][0], sws, s2, ST1_OFF + 0);
    SB();
    mfma16<false>(acc[7], fc[1][7], fc[1][8]);
    SB();
    mfma16<false>(acc[6], fc[1][0], fc[1][14]);
    SB();
    mfma16<false>(acc[5], fc[1][0], fc[1][13]);
    SB();
    mfma16<false>(acc[7], fc[1][0], fc[1][15]);
    SB();
    mfma16<false>(acc[6], fc[0][3], fc[0][11]);
    fc[1][3] = lds16(aA + 1728);
    ald16o<1024>(st[1][1], sws, s2, ST1_OFF + 4096);
    SB();
    mfma16<false>(acc[1], fc[0][0], fc[0][9]);
    SB();
    mfma16<false>(acc[7], fc[0][3], fc[0][12]);
    fc[1][11] = lds16(aB + 2240);
    SB();
    mfma16<false>(acc[3], fc[0][1], fc[0][10]);
    SB();
    mfma16<false>(acc[4], fc[0][2], fc[0][10]);
    fc[1][0] = lds16(aA + 192);
    ald16o<1024>(st[1][2], sws, s2, ST1_OFF + 8192);
    SB();
    mfma16<false>(acc[2], fc[0][1], fc[0][9]);
    SB();
    mfma16<false>(acc[5], fc[0][3], fc[0][10]);
    fc[1][9] = lds16(aB + 3264);
    SB();
    mfma16<false>(acc[0], fc[0][0], fc[0][8]);
    SB();
    mfma16<false>(acc[7], fc[0][4], fc[0][11]);
    fc[1][12] = lds16(aB + 1728);
    SB();
    mfma16<false>(acc[6], fc[0][2], fc[0][12]);
    ald16o<1024>(st[1][3], sws, s2, ST1_OFF + 12288);
    SB();
    mfma16<false>(acc[5], fc[0][2], fc[0][11]);
    fc[1][1] = lds16(aA + 704);
    SB();
    mfma16<false>(acc[3], fc[0][2], fc[0][9]);
    SB();
    mfma16<false>(acc[7], fc[0][2], fc[0][13]);
    fc[1][10] = lds16(aB + 2752);
    SB();
    mfma16<false>(acc[4], fc[0][1], fc[0][11]);
    ald16o<1024>(qv[1], sws, s2, qso);
    SB();
    mfma16<false>(acc[5], fc[0][1], fc[0][12]);
    fc[1][2] = lds16(aA + 1216);
    SB();
    mfma16<false>(acc[1], fc[0][1], fc[0][8]);
    SB();
    mfma16<false>(acc[2], fc[0][2], fc[0][8]);
    fc[1][8] = lds16(aB + 3776);
    SB();
    mfma16<false>(acc[4], fc[0][0], fc[0][12]);
    SB();
    mfma16<false>(acc[6], fc[0][4], fc[0][10]);
    fc[1][4] = lds16(aA + 2240);
    lda<0, 0>(sws, l16, wso + 0);
    SB();
    mfma16<false>(acc[7], fc[0][1], fc[0][14]);
    SB();
    mfma16<false>(acc[4], fc[0][3], fc[0][9]);
    fc[1][13] = lds16(aB + 1216);
    SB();
    mfma16<false>(acc[3], fc[0][3], fc[0][8]);
    SB();
    mfma16<false>(acc[2], fc[0][0], fc[0][10]);
    fc[1][14] = lds16(aB + 704);
    lda<1, 1024>(sws, l16, wso + 0);
    SB();
    mfma16<false>(acc[6], fc[0][1], fc[0][13]);
    SB();
    mfma16<false>(acc[3], fc[0][0], fc[0][11]);
    fc[1][5] = lds16(aA + 2752);
    SB();
    mfma16<false>(acc[7], fc[0][5], fc[0][10]);
    SB();
    mfma16<false>(acc[6], fc[0][6], fc[0][8]);
    fc[1][6] = lds16(aA + 3264);
    lda<2, 2048>(sws, l16, wso + 0);
    SB();
    mfma16<false>(acc[5], fc[0][4], fc[0][9]);
    SB();
    mfma16<false>(acc[7], fc[0][6], fc[0][9]);
    fc[1][7] = lds16(aA + 3776);
    SB();
    mfma16<false>(acc[4], fc[0][4], fc[0][8]);
    SB();
    mfma16<false>(acc[6], fc[0][5], fc[0][9]);
    fc[1][15] = lds16(aB + 192);
    SB();
    mfma16<false>(acc[5], fc[0][5], fc[0][8]);
    lda<3, 3072>(sws, l16, wso + 0);
    SB();
    mfma16<false>(acc[7], fc[0][7], fc[0][8]);
    SB();
    mfma16<false>(acc[6], fc[0][0], fc[0][14]);
    SB();
    mfma16<false>(acc[5], fc[0][0], fc[0][13]);
    SB();
    mfma16<false>(acc[7], fc[0][0], fc[0][15]);
    lda<4, 0>(sws, l16, wso + 4096);
    SB();
    mfma16<false>(acc[6], fc[1][3], fc[1][11]);
    SB();
    mfma16<false>(acc[1], fc[1][0], fc[1][9]);
    SB();
    mfma16<false>(acc[7], fc[1][3], fc[1][12]);
    SB();
    mfma16<false>(acc[3], fc[1][1], fc[1][10]);
    SB();
    mfma16<false>(acc[4], fc[1][2], fc[1][10]);
    WAITV(15);
    pin(mraw); pin(graw); pin(draw); _Pragma("unroll") for (int m = 0; m < 8; ++m) pin(mp[m]);
    lda<5, 1024>(sws, l16, wso + 4096);
    SB();
    mfma16<false>(acc[2], fc[1][1], fc[1][9]);
    SB();
    mfma16<false>(acc[5], fc[1][3], fc[1][10]);
    gq = dppadd<0xB1>(f_(graw)); mq = dppadd<0xB1>(f_(mraw)); pin(gq); pin(mq);
    SB();
    mfma16<false>(acc[0], fc[1][0], fc[1][8]);
    SB();
    mfma16<false>(acc[7], fc[1][4], fc[1][11]);
    gq = dppadd<0x4E>(gq); mq = dppadd<0x4E>(mq); pin(gq); pin(mq);
    lda<6, 2048>(sws, l16, wso + 4096);
    SB();
    mfma16<false>(acc[6], fc[1][2], fc[1][12]);
    SB();
    mfma16<false>(acc[5], fc[1][2], fc[1][11]);
    mq = rbf(mq); pin(mq);
    SB();
    mfma16<false>(acc[3], fc[1][2], fc[1][9]);
    SB();
    mfma16<false>(acc[7], fc[1][2], fc[1][13]);
    gq = rbf(gq); pin(gq);
    lda<7, 3072>(sws, l16, wso + 4096);
    SB();
    mfma16<false>(acc[4], fc[1][1], fc[1][11]);
    SB();
    mfma16<false>(acc[5], fc[1][1], fc[1][12]);
    *(LAS float*)gbw = gq; dv = lo16(draw); pin(dv);
    SB();
    mfma16<false>(acc[1], fc[1][1], fc[1][8]);
    SB();
    mfma16<false>(acc[2], fc[1][2], fc[1][8]);
    sa = f_(__builtin_amdgcn_readlane(u_(mq), 0)); ga = f_(__builtin_amdgcn_readlane(u_(mq), 8)); pins(sa); pins(ga);
    SB();
    mfma16<false>(acc[4], fc[1][0], fc[1][12]);
    lda<8, 0>(sws, l16, wso + 8192);
    SB();
    mfma16<false>(acc[6], fc[1][4], fc[1][10]);
    a1 = 1.f + f_(__builtin_amdgcn_readlane(u_(mq), 4)); pin(a1);
    SB();
    mfma16<false>(acc[7], fc[1][1], fc[1][14]);
    SB();
    mfma16<false>(acc[4], fc[1][3], fc[1][9]);
    sg = 1.f + f_(__builtin_amdgcn_readlane(u_(mq), 12)); pin(sg);
    SB();
    mfma16<false>(acc[3], fc[1][3], fc[1][8]);
    lda<9, 1024>(sws, l16, wso + 8192);
    SB();
    mfma16<false>(acc[2], fc[1][0], fc[1][10]);
    a1 = rbf(a1); pin(a1);
    SB();
    mfma16<false>(acc[6], fc[1][1], fc[1][13]);
    SB();
    mfma16<false>(acc[3], fc[1][0], fc[1][11]);
    sg = rbf(sg); pin(sg);
    SB();
    mfma16<false>(acc[7], fc[1][5], fc[1][10]);
    SB();
    mfma16<false>(acc[6], fc[1][6], fc[1][8]);
    xd[0][0] = trr(xda); xd[0][1] = trr(xda + 1024);
    lda<10, 2048>(sws, l16, wso + 8192);
    SB();
    mfma16<false>(acc[5], fc[1][4], fc[1][9]);
    SB();
    mfma16<false>(acc[7], fc[1][6], fc[1][9]);
    xd[1][0] = trr(xda ^ 64); xd[1][1] = trr((xda ^ 64) + 1024);
    SB();
    mfma16<false>(acc[4], fc[1][4], fc[1][8]);
    SB();
    mfma16<false>(acc[6], fc[1][5], fc[1][9]);
    if (is_stat) { v2f cb = {0, 0}, sb = {0, 0}; _Pragma("unroll") for (int m = 0; m < 4; ++m) { cb += __builtin_bit_cast(v2f, mp[m]); sb += __builtin_bit_cast(v2f, mp[4 + m]); }
      *(LAS u32*)(SIG_L + t * 4) = pk(unpk(pk(cb)) + 1.f); *(LAS u32*)(SIG_L + 1024 + t * 4) = pk(sb); }
    lda<11, 3072>(sws, l16, wso + 8192);
    SB();
    mfma16<false>(acc[5], fc[1][5], fc[1][8]);
    SB();
    mfma16<false>(acc[7], fc[1][7], fc[1][8]);
    SB();
    mfma16<false>(acc[6], fc[1][0], fc[1][14]);
    SB();
    mfma16<false>(acc[5], fc[1][0], fc[1][13]);
    SB();
    mfma16<false>(acc[7], fc[1][0], fc[1][15]);
    SB();
    asm volatile("s_nop 7\n\ts_nop 1" : "+v"(acc[0]), "+v"(acc[1]), "+v"(acc[2]), "+v"(acc[3]), "+v"(acc[4]), "+v"(acc[5]), "+v"(acc[6]), "+v"(acc[7])::"memory");
    if (kh) {
      sts16(rdw + 0, __builtin_bit_cast(v4u, acc[0])); lda<12, 0>(sws, l16, wso + 12288); SB();
      sts16(rdw + 1024, __builtin_bit_cast(v4u, acc[1])); lda<13, 1024>(sws, l16, wso + 12288); SB();
      sts16(rdw + 2048, __builtin_bit_cast(v4u, acc[2])); lda<14, 2048>(sws, l16, wso + 12288); SB();
      sts16(rdw + 3072, __builtin_bit_cast(v4u, acc[3])); lda<15, 3072>(sws, l16, wso + 12288); SB();
      lda<16, 0>(sws, l16, wso + 16384); lda<17, 1024>(sws, l16, wso + 16384); BAR();
      _Pragma("unroll") for (int m = 0; m < 4; ++m) y[m] = acc[4 + m] + __builtin_bit_cast(v4f, lds16(rdr + m * 1024));
      pin(y[0]); pin(y[1]); pin(y[2]); pin(y[3]); }
    else {
      sts16(rdw + 0, __builtin_bit_cast(v4u, acc[4])); lda<12, 0>(sws, l16, wso + 12288); SB();
      sts16(rdw + 1024, __builtin_bit_cast(v4u, acc[5])); lda<13, 1024>(sws, l16, wso + 12288); SB();
      sts16(rdw + 2048, __builtin_bit_cast(v4u, acc[6])); lda<14, 2048>(sws, l16, wso + 12288); SB();
      sts16(rdw + 3072, __builtin_bit_cast(v4u, acc[7])); lda<15, 3072>(sws, l16, wso + 12288); SB();
      lda<16, 0>(sws, l16, wso + 16384); lda<17, 1024>(sws, l16, wso + 16384); BAR();
      _Pragma("unroll") for (int m = 0; m < 4; ++m) y[m] = acc[0 + m] + __builtin_bit_cast(v4f, lds16(rdr + m * 1024));
      pin(y[0]); pin(y[1]); pin(y[2]); pin(y[3]); }
    PAGE(epi);
    WAITV(18);
    pin(qv[0]); pin(qv[1]); pin(st[0][0]); pin(st[0][1]); pin(st[0][2]); pin(st[0][3]); pin(st[1][0]); pin(st[1][1]); pin(st[1][2]); pin(st[1][3]);
    v4u o1[2], o2[2];
    st16c(cat(xd[0][0], xd[0][1]), rws, is_stat ? 0x80000000u : l16, wdo); st16c(cat(xd[1][0], xd[1][1]), rws, is_stat ? 0x80000000u : l16, wdo + 1024); SB();
    { const v4f y0 = y[0], y1 = y[1]; const v4u uw = lds16(uwa + 1024); float yy[8];
      _Pragma("unroll") for (int e = 0; e < 4; ++e) { const float ya = y0[e], yb = y1[e]; auto r = __builtin_amdgcn_permlane16_swap(u_(ya), u_(yb), false, false); yy[e] = f_(r[0]); yy[4 + e] = f_(r[1]); }
      o1[0][0] = mix1<0>(qv[0], uw, yy, st[0], sa, a1, ga, dv);
      lda<18, 2048>(sws, l16, wso + 16384); SB();
      o2[0][0] = pk(unpk(o1[0][0]) * sg);
      o1[0][1] = mix1<1>(qv[0], uw, yy, st[0], sa, a1, ga, dv);
      lda<19, 3072>(sws, l16, wso + 16384); SB();
      o2[0][1] = pk(unpk(o1[0][1]) * sg);
      o1[0][2] = mix1<2>(qv[0], uw, yy, st[0], sa, a1, ga, dv);
      o2[0][2] = pk(unpk(o1[0][2]) * sg);
      o1[0][3] = mix1<3>(qv[0], uw, yy, st[0], sa, a1, ga, dv);
      o2[0][3] = pk(unpk(o1[0][3]) * sg);
    }
    st16c(o1[0], rws, s2 + 0, xso); st16c(o2[0], rws, s2 + 0, xso + (X1S_OFF - X1_OFF));
    SB();
    { const v4f y0 = y[2], y1 = y[3]; const v4u uw = lds16(uwa + 0); float yy[8];
      _Pragma("unroll") for (int e = 0; e < 4; ++e) { const float ya = y0[e], yb = y1[e]; auto r = __builtin_amdgcn_permlane16_swap(u_(ya), u_(yb), false, false); yy[e] = f_(r[0]); yy[4 + e] = f_(r[1]); }
      o1[1][0] = mix1<0>(qv[1], uw, yy, st[1], sa, a1, ga, dv);
      lda<20, 0>(sws, l16, wso + 20480); SB();
      o2[1][0] = pk(unpk(o1[1][0]) * sg);
      o1[1][1] = mix1<1>(qv[1], uw, yy, st[1], sa, a1, ga, dv);
      lda<21, 1024>(sws, l16, wso + 20480); SB();
      o2[1][1] = pk(unpk(o1[1][1]) * sg);
      o1[1][2] = mix1<2>(qv[1], uw, yy, st[1], sa, a1, ga, dv);
      o2[1][2] = pk(unpk(o1[1][2]) * sg);
      o1[1][3] = mix1<3>(qv[1], uw, yy, st[1], sa, a1, ga, dv);
      o2[1][3] = pk(unpk(o1[1][3]) * sg);
    }
    st16c(o1[1], rws, s2 + 1024, xso); st16c(o2[1], rws, s2 + 1024, xso + (X1S_OFF - X1_OFF));
    SB();
  }

  auto res_tile = [&](int n) {
    const int c = (lane & 7) ^ ((g & 1) << 2);
    gldsc(sws, (lane >> 3) * RS + (c >> 2) * 2048 + (c & 3) * 16, X1_OFF + (64 * dt + 16 * w + 8 * n) * RS + 64 * mt2, X1L_L + (2 * w + n) * 1024);
  };
  const char* fc0 = P.ws + FLAGC_OFF + ((mt2 >> 1) * 56 + 14 * w + (i16 < 14 ? i16 : 13)) * FS;
  v4u wp[12];
  if (__builtin_expect(!is_stat, 1)) {
    u32 tb[2] = {tr64, tr64 ^ 64}; const char* pb = cnt_addr(CB);
    v16f acc[2][2][2]; v4s f[3][2][2]; v4u ta[2], sb[2], o[4]; float r2[2], m0, m1; u32 bad;
    const u32 ro = (lane & 31) * 16, to = ro + h * 1024, tso = TV_OFF + (nt * 4 + w) * 2048, sso = ST2_OFF + mt * 1024;
    auto again = [&](auto hc) { constexpr int hf = hc;
      if (__builtin_expect(!__all(bad == 0), 0)) {
        sb[0] = reread(rws, ro, sso + hf * 16384, rt); sb[1] = reread(rws, ro, sso + hf * 16384 + 512, rt);
        if constexpr (hf == 0) { ta[0] = reread(rws, to, tso, rt); ta[1] = reread(rws, to, tso + 512, rt); } }
      pin(sb[0]); pin(sb[1]); pin(ta[0]); pin(ta[1]); };
    u32 a_l = w * 1024, a_g = X1S_OFF + 8 * w * RS + 128 * mt; pins(a_l); pins(a_g);
    auto hh_off = [&](int hf, int cc) { return HH_OFF + ((32 * hf + 2 * mt + (cc & 1)) * 112 + 8 * nt + 2 * w + (cc >> 1)) * 1024; };
    WARM4(p1, p2, g2, g2);
    DRAIN(); asm volatile("s_barrier" ::: "memory");
    if (w == 0) add64(sws, lane < 8 ? shard : 0x80000000u, CB);
    lda<22, 2048>(sws, l16, wso + 20480);
    lda<23, 3072>(sws, l16, wso + 20480);
    lda<24, 0>(sws, l16, wso + 24576);
    lda<25, 1024>(sws, l16, wso + 24576);
    lda<26, 2048>(sws, l16, wso + 24576);
    lda<27, 3072>(sws, l16, wso + 24576);
    lda<28, 0>(sws, l16, wso + 28672);
    lda<29, 1024>(sws, l16, wso + 28672);
    lda<30, 2048>(sws, l16, wso + 28672);
    lda<31, 3072>(sws, l16, wso + 28672);
    lda<32, 0>(sws, l16, wso + 32768);
    lda<33, 1024>(sws, l16, wso + 32768);
    lda<34, 2048>(sws, l16, wso + 32768);
    lda<35, 3072>(sws, l16, wso + 32768);
    lda<36, 0>(sws, l16, wso + 36864);
    lda<37, 1024>(sws, l16, wso + 36864);
    lda<38, 2048>(sws, l16, wso + 36864);
    lda<39, 3072>(sws, l16, wso + 36864);
    lda<40, 0>(sws, l16, wso + 40960);
    lda<41, 1024>(sws, l16, wso + 40960);
    lda<42, 2048>(sws, l16, wso + 40960);
    lda<43, 3072>(sws, l16, wso + 40960);
    lda<44, 0>(sws, l16, wso + 45056);
    lda<45, 1024>(sws, l16, wso + 45056);
    if (w == 1) { peek(fl, pb); lda<46, 2048>(sws, l16, wso + 45056); lda<47, 3072>(sws, l16, wso + 45056); lda<48, 0>(sws, l16, wso + 49152); lda<49, 1024>(sws, l16, wso + 49152); lda<50, 2048>(sws, l16, wso + 49152); lda<51, 3072>(sws, l16, wso + 49152); check<6>(fl, pb, full); } else { lda<46, 2048>(sws, l16, wso + 45056); lda<47, 3072>(sws, l16, wso + 45056); lda<48, 0>(sws, l16, wso + 49152); lda<49, 1024>(sws, l16, wso + 49152); lda<50, 2048>(sws, l16, wso + 49152); lda<51, 3072>(sws, l16, wso + 49152); }
    asm volatile("s_barrier" ::: "memory");
    gldsk<0, 0>(sws, sw64g, a_g, a_l);
    gldsk<4096, 135168>(sws, sw64g, a_g, a_l);
    gldsk<8192, 270336>(sws, sw64g, a_g, a_l);
    gldsk<12288, 405504>(sws, sw64g, a_g, a_l);
    gldsk<16384, 540672>(sws, sw64g, a_g, a_l);
    waitbar<3>();
    f[0][0][0] = trr(tb[0] + 0);
    f[0][0][1] = trr(tb[0] + 512);
    f[0][1][0] = trr(tb[1] + 0);
    f[0][1][1] = trr(tb[1] + 512);
    f[1][0][0] = trr(tb[0] + 2048);
    f[1][0][1] = trr(tb[0] + 2560);
    f[1][1][0] = trr(tb[1] + 2048);
    f[1][1][1] = trr(tb[1] + 2560);
    SB();
    mfma32a<0, true>(acc[0][0][0], cat(f[0][0][0], f[0][0][1]));
    f[2][0][0] = trr(tb[0] + 4096);
    SB();
    mfma32a<1, true>(acc[0][1][0], cat(f[0][0][0], f[0][0][1]));
    f[2][0][1] = trr(tb[0] + 4608);
    gldsk<20480, 675840>(sws, sw64g, a_g, a_l);
    SB();
    mfma32a<0, true>(acc[0][0][1], cat(f[0][1][0], f[0][1][1]));
    f[2][1][0] = trr(tb[1] + 4096);
    SB();
    mfma32a<1, true>(acc[0][1][1], cat(f[0][1][0], f[0][1][1]));
    f[2][1][1] = trr(tb[1] + 4608);
    gldsk<24576, 811008>(sws, sw64g, a_g, a_l);
    SB();
    SB();
    mfma32a<2, false>(acc[0][0][0], cat(f[1][0][0], f[1][0][1]));
    f[0][0][0] = trr(tb[0] + 6144);
    SB();
    mfma32a<3, false>(acc[0][1][0], cat(f[1][0][0], f[1][0][1]));
    f[0][0][1] = trr(tb[0] + 6656);
    gldsk<28672, 946176>(sws, sw64g, a_g, a_l);
    SB();
    mfma32a<2, false>(acc[0][0][1], cat(f[1][1][0], f[1][1][1]));
    f[0][1][0] = trr(tb[1] + 6144);
    SB();
    mfma32a<3, false>(acc[0][1][1], cat(f[1][1][0], f[1][1][1]));
    f[0][1][1] = trr(tb[1] + 6656);
    gldsk<32768, 1081344>(sws, sw64g, a_g, a_l);
    SB();
    waitbar<4>();
    SB();
    mfma32a<4, false>(acc[0][0][0], cat(f[2][0][0], f[2][0][1]));
    f[1][0][0] = trr(tb[0] + 8192);
    SB();
    mfma32a<5, false>(acc[0][1][0], cat(f[2][0][0], f[2][0][1]));
    f[1][0][1] = trr(tb[0] + 8704);
    gldsk<36864, 1216512>(sws, sw64g, a_g, a_l);
    SB();
    mfma32a<4, false>(acc[0][0][1], cat(f[2][1][0], f[2][1][1]));
    f[1][1][0] = trr(tb[1] + 8192);
    SB();
    mfma32a<5, false>(acc[0][1][1], cat(f[2][1][0], f[2][1][1]));
    f[1][1][1] = trr(tb[1] + 8704);
    gldsk<40960, 1351680>(sws, sw64g, a_g, a_l);
    SB();
    SB();
    mfma32a<6, false>(acc[0][0][0], cat(f[0][0][0], f[0][0][1]));
    f[2][0][0] = trr(tb[0] + 10240);
    SB();
    mfma32a<7, false>(acc[0][1][0], cat(f[0][0][0], f[0][0][1]));
    f[2][0][1] = trr(tb[0] + 10752);
    gldsk<45056, 1486848>(sws, sw64g, a_g, a_l);
    SB();
    mfma32a<6, false>(acc[0][0][1], cat(f[0][1][0], f[0][1][1]));
    f[2][1][0] = trr(tb[1] + 10240);
    SB();
    mfma32a<7, false>(acc[0][1][1], cat(f[0][1][0], f[0][1][1]));
    f[2][1][1] = trr(tb[1] + 10752);
    gldsk<49152, 1622016>(sws, sw64g, a_g, a_l);
    SB();
    SB();
    mfma32a<8, false>(acc[0][0][0], cat(f[1][0][0], f[1][0][1]));
    f[0][0][0] = trr(tb[0] + 12288);
    SB();
    mfma32a<9, false>(acc[0][1][0], cat(f[1][0][0], f[1][0][1]));
    f[0][0][1] = trr(tb[0] + 12800);
    gldsk<53248, 1757184>(sws, sw64g, a_g, a_l);
    SB();
    mfma32a<8, false>(acc[0][0][1], cat(f[1][1][0], f[1][1][1]));
    f[0][1][0] = trr(tb[1] + 12288);
    SB();
    mfma32a<9, false>(acc[0][1][1], cat(f[1][1][0], f[1][1][1]));
    f[0][1][1] = trr(tb[1] + 12800);
    gldsk<57344, 1892352>(sws, sw64g, a_g, a_l);
    SB();
    SB();
    mfma32a<10, false>(acc[0][0][0], cat(f[2][0][0], f[2][0][1]));
    f[1][0][0] = trr(tb[0] + 14336);
    SB();
    mfma32a<11, false>(acc[0][1][0], cat(f[2][0][0], f[2][0][1]));
    f[1][0][1] = trr(tb[0] + 14848);
    gldsk<61440, 2027520>(sws, sw64g, a_g, a_l);
    SB();
    mfma32a<10, false>(acc[0][0][1], cat(f[2][1][0], f[2][1][1]));
    f[1][1][0] = trr(tb[1] + 14336);
    SB();
    mfma32a<11, false>(acc[0][1][1], cat(f[2][1][0], f[2][1][1]));
    f[1][1][1] = trr(tb[1] + 14848);
    lda<52, 0>(sws, l16, wso + 53248);
    SB();
    SB();
    mfma32a<12, false>(acc[0][0][0], cat(f[0][0][0], f[0][0][1]));
    f[2][0][0] = trr(tb[0] + 16384);
    SB();
    mfma32a<13, false>(acc[0][1][0], cat(f[0][0][0], f[0][0][1]));
    f[2][0][1] = trr(tb[0] + 16896);
    lda<53, 1024>(sws, l16, wso + 53248);
    SB();
    mfma32a<12, false>(acc[0][0][1], cat(f[0][1][0], f[0][1][1]));
    f[2][1][0] = trr(tb[1] + 16384);
    SB();
    mfma32a<13, false>(acc[0][1][1], cat(f[0][1][0], f[0][1][1]));
    f[2][1][1] = trr(tb[1] + 16896);
    lda<54, 2048>(sws, l16, wso + 53248);
    SB();
    SB();
    mfma32a<14, false>(acc[0][0][0], cat(f[1][0][0], f[1][0][1]));
    f[0][0][0] = trr(tb[0] + 18432);
    SB();
    mfma32a<15, false>(acc[0][1][0], cat(f[1][0][0], f[1][0][1]));
    f[0][0][1] = trr(tb[0] + 18944);
    lda<55, 3072>(sws, l16, wso + 53248);
    SB();
    mfma32a<14, false>(acc[0][0][1], cat(f[1][1][0], f[1][1][1]));
    f[0][1][0] = trr(tb[1] + 18432);
    SB();
    mfma32a<15, false>(acc[0][1][1], cat(f[1][1][0], f[1][1][1]));
    f[0][1][1] = trr(tb[1] + 18944);
    lda<56, 0>(sws, l16, wso + 57344);
    SB();
    waitbar<10>();
    SB();
    mfma32a<16, false>(acc[0][0][0], cat(f[2][0][0], f[2][0][1]));
    f[1][0][0] = trr(tb[0] + 20480);
    SB();
    mfma32a<17, false>(acc[0][1][0], cat(f[2][0][0], f[2][0][1]));
    f[1][0][1] = trr(tb[0] + 20992);
    lda<57, 1024>(sws, l16, wso + 57344);
    SB();
    mfma32a<16, false>(acc[0][0][1], cat(f[2][1][0], f[2][1][1]));
    f[1][1][0] = trr(tb[1] + 20480);
    SB();
    mfma32a<17, false>(acc[0][1][1], cat(f[2][1][0], f[2][1][1]));
    f[1][1][1] = trr(tb[1] + 20992);
    lda<58, 2048>(sws, l16, wso + 57344);
    SB();
    SB();
    mfma32a<18, false>(acc[0][0][0], cat(f[0][0][0], f[0][0][1]));
    f[2][0][0] = trr(tb[0] + 22528);
    SB();
    mfma32a<19, false>(acc[0][1][0], cat(f[0][0][0], f[0][0][1]));
    f[2][0][1] = trr(tb[0] + 23040);
    lda<59, 3072>(sws, l16, wso + 57344);
    SB();
    mfma32a<18, false>(acc[0][0][1], cat(f[0][1][0], f[0][1][1]));
    f[2][1][0] = trr(tb[1] + 22528);
    SB();
    mfma32a<19, false>(acc[0][1][1], cat(f[0][1][0], f[0][1][1]));
    f[2][1][1] = trr(tb[1] + 23040);
    lda<60, 0>(sws, l16, wso + 61440);
    SB();
    SB();
    mfma32a<20, false>(acc[0][0][0], cat(f[1][0][0], f[1][0][1]));
    f[0][0][0] = trr(tb[0] + 24576);
    SB();
    mfma32a<21, false>(acc[0][1][0], cat(f[1][0][0], f[1][0][1]));
    f[0][0][1] = trr(tb[0] + 25088);
    lda<61, 1024>(sws, l16, wso + 61440);
    SB();
    mfma32a<20, false>(acc[0][0][1], cat(f[1][1][0], f[1][1][1]));
    f[0][1][0] = trr(tb[1] + 24576);
    SB();
    mfma32a<21, false>(acc[0][1][1], cat(f[1][1][0], f[1][1][1]));
    f[0][1][1] = trr(tb[1] + 25088);
    lda<62, 2048>(sws, l16, wso + 61440);
    SB();
    SB();
    mfma32a<22, false>(acc[0][0][0], cat(f[2][0][0], f[2][0][1]));
    f[1][0][0] = trr(tb[0] + 26624);
    SB();
    mfma32a<23, false>(acc[0][1][0], cat(f[2][0][0], f[2][0][1]));
    f[1][0][1] = trr(tb[0] + 27136);
    lda<63, 3072>(sws, l16, wso + 61440);
    SB();
    mfma32a<22, false>(acc[0][0][1], cat(f[2][1][0], f[2][1][1]));
    f[1][1][0] = trr(tb[1] + 26624);
    SB();
    mfma32a<23, false>(acc[0][1][1], cat(f[2][1][0], f[2][1][1]));
    f[1][1][1] = trr(tb[1] + 27136);
    SB();
    SB();
    mfma32a<24, false>(acc[0][0][0], cat(f[0][0][0], f[0][0][1]));
    f[2][0][0] = trr(tb[0] + 28672);
    SB();
    mfma32a<25, false>(acc[0][1][0], cat(f[0][0][0], f[0][0][1]));
    f[2][0][1] = trr(tb[0] + 29184);
    gldsk<65536, 2048>(sws, sw64g, a_g, a_l);
    SB();
    mfma32a<24, false>(acc[0][0][1], cat(f[0][1][0], f[0][1][1]));
    f[2][1][0] = trr(tb[1] + 28672);
    SB();
    mfma32a<25, false>(acc[0][1][1], cat(f[0][1][0], f[0][1][1]));
    f[2][1][1] = trr(tb[1] + 29184);
    SB();
    SB();
    mfma32a<26, false>(acc[0][0][0], cat(f[1][0][0], f[1][0][1]));
    f[0][0][0] = trr(tb[0] + 30720);
    SB();
    mfma32a<27, false>(acc[0][1][0], cat(f[1][0][0], f[1][0][1]));
    f[0][0][1] = trr(tb[0] + 31232);
    gldsk<69632, 137216>(sws, sw64g, a_g, a_l);
    SB();
    mfma32a<26, false>(acc[0][0][1], cat(f[1][1][0], f[1][1][1]));
    f[0][1][0] = trr(tb[1] + 30720);
    SB();
    mfma32a<27, false>(acc[0][1][1], cat(f[1][1][0], f[1][1][1]));
    f[0][1][1] = trr(tb[1] + 31232);
    SB();
    SB();
    mfma32a<28, false>(acc[0][0][0], cat(f[2][0][0], f[2][0][1]));
    f[1][0][0] = trr(tb[0] + 32768);
    SB();
    mfma32a<29, false>(acc[0][1][0], cat(f[2][0][0], f[2][0][1]));
    f[1][0][1] = trr(tb[0] + 33280);
    gldsk<73728, 272384>(sws, sw64g, a_g, a_l);
    SB();
    mfma32a<28, false>(acc[0][0][1], cat(f[2][1][0], f[2][1][1]));
    f[1][1][0] = trr(tb[1] + 32768);
    SB();
    mfma32a<29, false>(acc[0][1][1], cat(f[2][1][0], f[2][1][1]));
    f[1][1][1] = trr(tb[1] + 33280);
    SB();
    SB();
    mfma32a<30, false>(acc[0][0][0], cat(f[0][0][0], f[0][0][1]));
    f[2][0][0] = trr(tb[0] + 34816);
    SB();
    mfma32a<31, false>(acc[0][1][0], cat(f[0][0][0], f[0][0][1]));
    f[2][0][1] = trr(tb[0] + 35328);
    gldsk<77824, 407552>(sws, sw64g, a_g, a_l);
    SB();
    mfma32a<30, false>(acc[0][0][1], cat(f[0][1][0], f[0][1][1]));
    f[2][1][0] = trr(tb[1] + 34816);
    SB();
    mfma32a<31, false>(acc[0][1][1], cat(f[0][1][0], f[0][1][1]));
    f[2][1][1] = trr(tb[1] + 35328);
    SB();
    SB();
    mfma32a<32, false>(acc[0][0][0], cat(f[1][0][0], f[1][0][1]));
    f[0][0][0] = trr(tb[0] + 36864);
    SB();
    mfma32a<33, false>(acc[0][1][0], cat(f[1][0][0], f[1][0][1]));
    f[0][0][1] = trr(tb[0] + 37376);
    gldsk<81920, 542720>(sws, sw64g, a_g, a_l);
    SB();
    mfma32a<32, false>(acc[0][0][1], cat(f[1][1][0], f[1][1][1]));
    f[0][1][0] = trr(tb[1] + 36864);
    SB();
    mfma32a<33, false>(acc[0][1][1], cat(f[1][1][0], f[1][1][1]));
    f[0][1][1] = trr(tb[1] + 37376);
    SB();
    SB();
    mfma32a<34, false>(acc[0][0][0], cat(f[2][0][0], f[2][0][1]));
    f[1][0][0] = trr(tb[0] + 38912);
    SB();
    mfma32a<35, false>(acc[0][1][0], cat(f[2][0][0], f[2][0][1]));
    f[1][0][1] = trr(tb[0] + 39424);
    gldsk<86016, 677888>(sws, sw64g, a_g, a_l);
    SB();
    mfma32a<34, false>(acc[0][0][1], cat(f[2][1][0], f[2][1][1]));
    f[1][1][0] = trr(tb[1] + 38912);
    SB();
    mfma32a<35, false>(acc[0][1][1], cat(f[2][1][0], f[2][1][1]));
    f[1][1][1] = trr(tb[1] + 39424);
    SB();
    waitbar<18>();
    PAGE(p1);
    SB();
    mfma32a<36, false>(acc[0][0][0], cat(f[0][0][0], f[0][0][1]));
    f[2][0][0] = trr(tb[0] + 40960);
    SB();
    mfma32a<37, false>(acc[0][1][0], cat(f[0][0][0], f[0][0][1]));
    f[2][0][1] = trr(tb[0] + 41472);
    gldsk<90112, 813056>(sws, sw64g, a_g, a_l);
    SB();
    mfma32a<36, false>(acc[0][0][1], cat(f[0][1][0], f[0][1][1]));
    f[2][1][0] = trr(tb[1] + 40960);
    SB();
    mfma32a<37, false>(acc[0][1][1], cat(f[0][1][0], f[0][1][1]));
    f[2][1][1] = trr(tb[1] + 41472);
    SB();
    SB();
    mfma32a<38, false>(acc[0][0][0], cat(f[1][0][0], f[1][0][1]));
    f[0][0][0] = trr(tb[0] + 43008);
    SB();
    mfma32a<39, false>(acc[0][1][0], cat(f[1][0][0], f[1][0][1]));
    f[0][0][1] = trr(tb[0] + 43520);
    gldsk<94208, 948224>(sws, sw64g, a_g, a_l);
    SB();
    mfma32a<38, false>(acc[0][0][1], cat(f[1][1][0], f[1][1][1]));
    f[0][1][0] = trr(tb[1] + 43008);
    SB();
    mfma32a<39, false>(acc[0][1][1], cat(f[1][1][0], f[1][1][1]));
    f[0][1][1] = trr(tb[1] + 43520);
    SB();
    SB();
    mfma32a<40, false>(acc[0][0][0], cat(f[2][0][0], f[2][0][1]));
    f[1][0][0] = trr(tb[0] + 45056);
    SB();
    mfma32a<41, false>(acc[0][1][0], cat(f[2][0][0], f[2][0][1]));
    f[1][0][1] = trr(tb[0] + 45568);
    gldsk<98304, 1083392>(sws, sw64g, a_g, a_l);
    SB();
    mfma32a<40, false>(acc[0][0][1], cat(f[2][1][0], f[2][1][1]));
    f[1][1][0] = trr(tb[1] + 45056);
    SB();
    mfma32a<41, false>(acc[0][1][1], cat(f[2][1][0], f[2][1][1]));
    f[1][1][1] = trr(tb[1] + 45568);
    SB();
    SB();
    mfma32a<42, false>(acc[0][0][0], cat(f[0][0][0], f[0][0][1]));
    f[2][0][0] = trr(tb[0] + 47104);
    SB();
    mfma32a<43, false>(acc[0][1][0], cat(f[0][0][0], f[0][0][1]));
    f[2][0][1] = trr(tb[0] + 47616);
    gldsk<102400, 1218560>(sws, sw64g, a_g, a_l);
    SB();
    mfma32a<42, false>(acc[0][0][1], cat(f[0][1][0], f[0][1][1]));
    f[2][1][0] = trr(tb[1] + 47104);
    SB();
    mfma32a<43, false>(acc[0][1][1], cat(f[0][1][0], f[0][1][1]));
    f[2][1][1] = trr(tb[1] + 47616);
    SB();
    SB();
    mfma32a<44, false>(acc[0][0][0], cat(f[1][0][0], f[1][0][1]));
    f[0][0][0] = trr(tb[0] + 49152);
    gldsk<106496, 1353728>(sws, sw64g, a_g, a_l);
    SB();
    mfma32a<45, false>(acc[0][1][0], cat(f[1][0][0], f[1][0][1]));
    f[0][0][1] = trr(tb[0] + 49664);
    ald16o<0>(wp[0], sws, l16, wdso + 45056);
    SB();
    mfma32a<44, false>(acc[0][0][1], cat(f[1][1][0], f[1][1][1]));
    f[0][1][0] = trr(tb[1] + 49152);
    SB();
    mfma32a<45, false>(acc[0][1][1], cat(f[1][1][0], f[1][1][1]));
    f[0][1][1] = trr(tb[1] + 49664);
    ald16o<1024>(wp[1], sws, l16, wdso + 45056);
    SB();
    SB();
    mfma32a<46, false>(acc[0][0][0], cat(f[2][0][0], f[2][0][1]));
    f[1][0][0] = trr(tb[0] + 51200);
    gldsk<110592, 1488896>(sws, sw64g, a_g, a_l);
    SB();
    mfma32a<47, false>(acc[0][1][0], cat(f[2][0][0], f[2][0][1]));
    f[1][0][1] = trr(tb[0] + 51712);
    ald16o<2048>(wp[2], sws, l16, wdso + 45056);
    SB();
    mfma32a<46, false>(acc[0][0][1], cat(f[2][1][0], f[2][1][1]));
    f[1][1][0] = trr(tb[1] + 51200);
    SB();
    mfma32a<47, false>(acc[0][1][1], cat(f[2][1][0], f[2][1][1]));
    f[1][1][1] = trr(tb[1] + 51712);
    ald16o<3072>(wp[3], sws, l16, wdso + 45056);
    SB();
    SB();
    mfma32a<48, false>(acc[0][0][0], cat(f[0][0][0], f[0][0][1]));
    f[2][0][0] = trr(tb[0] + 53248);
    gldsk<114688, 1624064>(sws, sw64g, a_g, a_l);
    SB();
    mfma32a<49, false>(acc[0][1][0], cat(f[0][0][0], f[0][0][1]));
    f[2][0][1] = trr(tb[0] + 53760);
    ald16o<0>(wp[4], sws, l16, wdso + 49152);
    SB();
    mfma32a<48, false>(acc[0][0][1], cat(f[0][1][0], f[0][1][1]));
    f[2][1][0] = trr(tb[1] + 53248);
    SB();
    mfma32a<49, false>(acc[0][1][1], cat(f[0][1][0], f[0][1][1]));
    f[2][1][1] = trr(tb[1] + 53760);
    ald16o<1024>(wp[5], sws, l16, wdso + 49152);
    SB();
    SB();
    mfma32a<50, false>(acc[0][0][0], cat(f[1][0][0], f[1][0][1]));
    f[0][0][0] = trr(tb[0] + 55296);
    gldsk<118784, 1759232>(sws, sw64g, a_g, a_l);
    SB();
    mfma32a<51, false>(acc[0][1][0], cat(f[1][0][0], f[1][0][1]));
    f[0][0][1] = trr(tb[0] + 55808);
    ald16o<2048>(wp[6], sws, l16, wdso + 49152);
    SB();
    mfma32a<50, false>(acc[0][0][1], cat(f[1][1][0], f[1][1][1]));
    f[0][1][0] = trr(tb[1] + 55296);
    SB();
    mfma32a<51, false>(acc[0][1][1], cat(f[1][1][0], f[1][1][1]));
    f[0][1][1] = trr(tb[1] + 55808);
    ald16o<3072>(wp[7], sws, l16, wdso + 49152);
    SB();
    WAITV(32);
    SB();
    mfma32a<52, false>(acc[0][0][0], cat(f[2][0][0], f[2][0][1]));
    f[1][0][0] = trr(tb[0] + 57344);
    gldsk<122880, 1894400>(sws, sw64g, a_g, a_l);
    SB();
    mfma32a<53, false>(acc[0][1][0], cat(f[2][0][0], f[2][0][1]));
    f[1][0][1] = trr(tb[0] + 57856);
    ald16o<0>(ta[0], sws, to, tso);
    SB();
    mfma32a<52, false>(acc[0][0][1], cat(f[2][1][0], f[2][1][1]));
    f[1][1][0] = trr(tb[1] + 57344);
    SB();
    mfma32a<53, false>(acc[0][1][1], cat(f[2][1][0], f[2][1][1]));
    f[1][1][1] = trr(tb[1] + 57856);
    ald16o<512>(ta[1], sws, to, tso);
    SB();
    WAITV(33);
    SB();
    mfma32a<54, false>(acc[0][0][0], cat(f[0][0][0], f[0][0][1]));
    f[2][0][0] = trr(tb[0] + 59392);
    gldsk<126976, 2029568>(sws, sw64g, a_g, a_l);
    SB();
    mfma32a<55, false>(acc[0][1][0], cat(f[0][0][0], f[0][0][1]));
    f[2][0][1] = trr(tb[0] + 59904);
    ald16o<0>(sb[0], sws, ro, sso);
    SB();
    mfma32a<54, false>(acc[0][0][1], cat(f[0][1][0], f[0][1][1]));
    f[2][1][0] = trr(tb[1] + 59392);
    SB();
    mfma32a<55, false>(acc[0][1][1], cat(f[0][1][0], f[0][1][1]));
    f[2][1][1] = trr(tb[1] + 59904);
    ald16o<512>(sb[1], sws, ro, sso);
    SB();
    WAITV(34);
    SB();
    mfma32a<56, false>(acc[0][0][0], cat(f[1][0][0], f[1][0][1]));
    f[0][0][0] = trr(tb[0] + 61440);
    SB();
    mfma32a<57, false>(acc[0][1][0], cat(f[1][0][0], f[1][0][1]));
    f[0][0][1] = trr(tb[0] + 61952);
    ald16o<0>(wp[8], sws, l16, wdso + 53248);
    SB();
    mfma32a<56, false>(acc[0][0][1], cat(f[1][1][0], f[1][1][1]));
    f[0][1][0] = trr(tb[1] + 61440);
    SB();
    mfma32a<57, false>(acc[0][1][1], cat(f[1][1][0], f[1][1][1]));
    f[0][1][1] = trr(tb[1] + 61952);
    ald16o<1024>(wp[9], sws, l16, wdso + 53248);
    SB();
    WAITV(34);
    SB();
    mfma32a<58, false>(acc[0][0][0], cat(f[2][0][0], f[2][0][1]));
    f[1][0][0] = trr(tb[0] + 63488);
    SB();
    mfma32a<59, false>(acc[0][1][0], cat(f[2][0][0], f[2][0][1]));
    f[1][0][1] = trr(tb[0] + 64000);
    ald16o<2048>(wp[10], sws, l16, wdso + 53248);
    SB();
    mfma32a<58, false>(acc[0][0][1], cat(f[2][1][0], f[2][1][1]));
    f[1][1][0] = trr(tb[1] + 63488);
    SB();
    mfma32a<59, false>(acc[0][1][1], cat(f[2][1][0], f[2][1][1]));
    f[1][1][1] = trr(tb[1] + 64000);
    ald16o<3072>(wp[11], sws, l16, wdso + 53248);
    SB();
    WAITV(34);
    waitbar<27>();
    tb[0] += 65536; tb[1] += 65536; pin2(tb[0], tb[1]);
    SB();
    mfma32a<60, false>(acc[0][0][0], cat(f[0][0][0], f[0][0][1]));
    f[2][0][0] = trr(tb[0] + 0);
    SB();
    mfma32a<61, false>(acc[0][1][0], cat(f[0][0][0], f[0][0][1]));
    f[2][0][1] = trr(tb[0] + 512);
    SB();
    mfma32a<60, false>(acc[0][0][1], cat(f[0][1][0], f[0][1][1]));
    f[2][1][0] = trr(tb[1] + 0);
    SB();
    mfma32a<61, false>(acc[0][1][1], cat(f[0][1][0], f[0][1][1]));
    f[2][1][1] = trr(tb[1] + 512);
    SB();
    WAITV(32);
    SB();
    mfma32a<62, false>(acc[0][0][0], cat(f[1][0][0], f[1][0][1]));
    f[0][0][0] = trr(tb[0] + 2048);
    SB();
    mfma32a<63, false>(acc[0][1][0], cat(f[1][0][0], f[1][0][1]));
    f[0][0][1] = trr(tb[0] + 2560);
    res_tile(0);
    SB();
    mfma32a<62, false>(acc[0][0][1], cat(f[1][1][0], f[1][1][1]));
    f[0][1][0] = trr(tb[1] + 2048);
    SB();
    mfma32a<63, false>(acc[0][1][1], cat(f[1][1][0], f[1][1][1]));
    f[0][1][1] = trr(tb[1] + 2560);
    res_tile(1);
    SB();
    SB();
    mfma32a<0, true>(acc[1][0][0], cat(f[2][0][0], f[2][0][1]));
    f[1][0][0] = trr(tb[0] + 4096);
    SB();
    mfma32a<1, true>(acc[1][1][0], cat(f[2][0][0], f[2][0][1]));
    f[1][0][1] = trr(tb[0] + 4608);
    SB();
    mfma32a<0, true>(acc[1][0][1], cat(f[2][1][0], f[2][1][1]));
    f[1][1][0] = trr(tb[1] + 4096);
    SB();
    mfma32a<1, true>(acc[1][1][1], cat(f[2][1][0], f[2][1][1]));
    f[1][1][1] = trr(tb[1] + 4608);
    SB();
    SB();
    mfma32a<2, false>(acc[1][0][0], cat(f[0][0][0], f[0][0][1]));
    f[2][0][0] = trr(tb[0] + 6144);
    SB();
    mfma32a<3, false>(acc[1][1][0], cat(f[0][0][0], f[0][0][1]));
    f[2][0][1] = trr(tb[0] + 6656);
    lda<0, 0>(sws, l16, wdso + 0);
    SB();
    mfma32a<2, false>(acc[1][0][1], cat(f[0][1][0], f[0][1][1]));
    f[2][1][0] = trr(tb[1] + 6144);
    SB();
    mfma32a<3, false>(acc[1][1][1], cat(f[0][1][0], f[0][1][1]));
    f[2][1][1] = trr(tb[1] + 6656);
    lda<1, 1024>(sws, l16, wdso + 0);
    SB();
    SB();
    mfma32a<4, false>(acc[1][0][0], cat(f[1][0][0], f[1][0][1]));
    f[0][0][0] = trr(tb[0] + 8192);
    SB();
    mfma32a<5, false>(acc[1][1][0], cat(f[1][0][0], f[1][0][1]));
    f[0][0][1] = trr(tb[0] + 8704);
    lda<2, 2048>(sws, l16, wdso + 0);
    SB();
    mfma32a<4, false>(acc[1][0][1], cat(f[1][1][0], f[1][1][1]));
    f[0][1][0] = trr(tb[1] + 8192);
    SB();
    mfma32a<5, false>(acc[1][1][1], cat(f[1][1][0], f[1][1][1]));
    f[0][1][1] = trr(tb[1] + 8704);
    SB();
    WAITV(9);
    pin(sb[0]); pin(sb[1]); pin(ta[0]); pin(ta[1]);
    SB();
    mfma32a<6, false>(acc[1][0][0], cat(f[2][0][0], f[2][0][1]));
    f[1][0][0] = trr(tb[0] + 10240);
    bad = stale(sb[0], rt, 0); pin(bad);
    SB();
    mfma32a<7, false>(acc[1][1][0], cat(f[2][0][0], f[2][0][1]));
    f[1][0][1] = trr(tb[0] + 10752);
    bad = stale(sb[1], rt, bad); pin(bad);
    lda<3, 3072>(sws, l16, wdso + 0);
    SB();
    mfma32a<6, false>(acc[1][0][1], cat(f[2][1][0], f[2][1][1]));
    f[1][1][0] = trr(tb[1] + 10240);
    bad = stale(ta[0], rt, bad); pin(bad);
    SB();
    mfma32a<7, false>(acc[1][1][1], cat(f[2][1][0], f[2][1][1]));
    f[1][1][1] = trr(tb[1] + 10752);
    bad = stale(ta[1], rt, bad); pin(bad); again(std::integral_constant<int, 0>{});
    SB();
    SB();
    mfma32a<8, false>(acc[1][0][0], cat(f[0][0][0], f[0][0][1]));
    f[2][0][0] = trr(tb[0] + 12288);
    r2[0] = __builtin_amdgcn_rcpf(lo16(sb[0].z)); sb[0].y = 0; pin(r2[0]); pin(sb[0]);
    SB();
    mfma32a<9, false>(acc[1][1][0], cat(f[0][0][0], f[0][0][1]));
    f[2][0][1] = trr(tb[0] + 12800);
    r2[0] *= r2[0]; sb[0].w = 0; pin(r2[0]); pin(sb[0]);
    lda<4, 0>(sws, l16, wdso + 4096);
    SB();
    mfma32a<8, false>(acc[1][0][1], cat(f[0][1][0], f[0][1][1]));
    f[2][1][0] = trr(tb[1] + 12288);
    r2[1] = __builtin_amdgcn_rcpf(lo16(sb[1].z)); sb[1].y = 0; pin(r2[1]); pin(sb[1]);
    SB();
    mfma32a<9, false>(acc[1][1][1], cat(f[0][1][0], f[0][1][1]));
    f[2][1][1] = trr(tb[1] + 12800);
    r2[1] *= r2[1]; sb[1].w = 0; pin(r2[1]); pin(sb[1]);
    lda<5, 1024>(sws, l16, wdso + 4096);
    SB();
    SB();
    mfma32a<10, false>(acc[1][0][0], cat(f[1][0][0], f[1][0][1]));
    f[0][0][0] = trr(tb[0] + 14336);
    mfma32<false>(acc[0][0][0], ta[0], sb[0]);
    SB();
    mfma32a<11, false>(acc[1][1][0], cat(f[1][0][0], f[1][0][1]));
    f[0][0][1] = trr(tb[0] + 14848);
    mfma32<false>(acc[0][1][0], ta[1], sb[0]);
    lda<6, 2048>(sws, l16, wdso + 4096);
    SB();
    mfma32a<10, false>(acc[1][0][1], cat(f[1][1][0], f[1][1][1]));
    f[0][1][0] = trr(tb[1] + 14336);
    mfma32<false>(acc[0][0][1], ta[0], sb[1]);
    SB();
    mfma32a<11, false>(acc[1][1][1], cat(f[1][1][0], f[1][1][1]));
    f[0][1][1] = trr(tb[1] + 14848);
    mfma32<false>(acc[0][1][1], ta[1], sb[1]);
    SB();
    waitbar<15>();
    SB();
    mfma32a<12, false>(acc[1][0][0], cat(f[2][0][0], f[2][0][1]));
    f[1][0][0] = trr(tb[0] + 16384);
    gu1(m0, m1, acc[0][0][0][0], acc[0][0][0][8], acc[0][0][0][1], acc[0][0][0][9], r2[0]);
    SB();
    mfma32a<13, false>(acc[1][1][0], cat(f[2][0][0], f[2][0][1]));
    f[1][0][1] = trr(tb[0] + 16896);
    o[0][0] = gu2(m0, m1, r2[0]);
    lda<7, 3072>(sws, l16, wdso + 4096);
    SB();
    mfma32a<12, false>(acc[1][0][1], cat(f[2][1][0], f[2][1][1]));
    f[1][1][0] = trr(tb[1] + 16384);
    gu1(m0, m1, acc[0][0][0][2], acc[0][0][0][10], acc[0][0][0][3], acc[0][0][0][11], r2[0]);
    SB();
    mfma32a<13, false>(acc[1][1][1], cat(f[2][1][0], f[2][1][1]));
    f[1][1][1] = trr(tb[1] + 16896);
    o[0][1] = gu2(m0, m1, r2[0]);
    SB();
    SB();
    mfma32a<14, false>(acc[1][0][0], cat(f[0][0][0], f[0][0][1]));
    f[2][0][0] = trr(tb[0] + 18432);
    gu1(m0, m1, acc[0][0][0][4], acc[0][0][0][12], acc[0][0][0][5], acc[0][0][0][13], r2[0]);
    SB();
    mfma32a<15, false>(acc[1][1][0], cat(f[0][0][0], f[0][0][1]));
    f[2][0][1] = trr(tb[0] + 18944);
    o[0][2] = gu2(m0, m1, r2[0]);
    lda<8, 0>(sws, l16, wdso + 8192);
    SB();
    mfma32a<14, false>(acc[1][0][1], cat(f[0][1][0], f[0][1][1]));
    f[2][1][0] = trr(tb[1] + 18432);
    gu1(m0, m1, acc[0][0][0][6], acc[0][0][0][14], acc[0][0][0][7], acc[0][0][0][15], r2[0]);
    SB();
    mfma32a<15, false>(acc[1][1][1], cat(f[0][1][0], f[0][1][1]));
    f[2][1][1] = trr(tb[1] + 18944);
    o[0][3] = gu2(m0, m1, r2[0]);
    lda<9, 1024>(sws, l16, wdso + 8192);
    SB();
    SB();
    mfma32a<16, false>(acc[1][0][0], cat(f[1][0][0], f[1][0][1]));
    f[0][0][0] = trr(tb[0] + 20480);
    gu1(m0, m1, acc[0][0][1][0], acc[0][0][1][8], acc[0][0][1][1], acc[0][0][1][9], r2[1]);
    SB();
    mfma32a<17, false>(acc[1][1][0], cat(f[1][0][0], f[1][0][1]));
    f[0][0][1] = trr(tb[0] + 20992);
    o[1][0] = gu2(m0, m1, r2[1]);
    lda<10, 2048>(sws, l16, wdso + 8192);
    SB();
    mfma32a<16, false>(acc[1][0][1], cat(f[1][1][0], f[1][1][1]));
    f[0][1][0] = trr(tb[1] + 20480);
    gu1(m0, m1, acc[0][0][1][2], acc[0][0][1][10], acc[0][0][1][3], acc[0][0][1][11], r2[1]);
    SB();
    mfma32a<17, false>(acc[1][1][1], cat(f[1][1][0], f[1][1][1]));
    f[0][1][1] = trr(tb[1] + 20992);
    o[1][1] = gu2(m0, m1, r2[1]);
    SB();
    SB();
    mfma32a<18, false>(acc[1][0][0], cat(f[2][0][0], f[2][0][1]));
    f[1][0][0] = trr(tb[0] + 22528);
    gu1(m0, m1, acc[0][0][1][4], acc[0][0][1][12], acc[0][0][1][5], acc[0][0][1][13], r2[1]);
    SB();
    mfma32a<19, false>(acc[1][1][0], cat(f[2][0][0], f[2][0][1]));
    f[1][0][1] = trr(tb[0] + 23040);
    o[1][2] = gu2(m0, m1, r2[1]);
    lda<11, 3072>(sws, l16, wdso + 8192);
    SB();
    mfma32a<18, false>(acc[1][0][1], cat(f[2][1][0], f[2][1][1]));
    f[1][1][0] = trr(tb[1] + 22528);
    gu1(m0, m1, acc[0][0][1][6], acc[0][0][1][14], acc[0][0][1][7], acc[0][0][1][15], r2[1]);
    SB();
    mfma32a<19, false>(acc[1][1][1], cat(f[2][1][0], f[2][1][1]));
    f[1][1][1] = trr(tb[1] + 23040);
    o[1][3] = gu2(m0, m1, r2[1]);
    SB();
    SB();
    mfma32a<20, false>(acc[1][0][0], cat(f[0][0][0], f[0][0][1]));
    f[2][0][0] = trr(tb[0] + 24576);
    gu1(m0, m1, acc[0][1][0][0], acc[0][1][0][8], acc[0][1][0][1], acc[0][1][0][9], r2[0]);
    SB();
    mfma32a<21, false>(acc[1][1][0], cat(f[0][0][0], f[0][0][1]));
    f[2][0][1] = trr(tb[0] + 25088);
    o[2][0] = gu2(m0, m1, r2[0]);
    lda<12, 0>(sws, l16, wdso + 12288);
    SB();
    mfma32a<20, false>(acc[1][0][1], cat(f[0][1][0], f[0][1][1]));
    f[2][1][0] = trr(tb[1] + 24576);
    gu1(m0, m1, acc[0][1][0][2], acc[0][1][0][10], acc[0][1][0][3], acc[0][1][0][11], r2[0]);
    SB();
    mfma32a<21, false>(acc[1][1][1], cat(f[0][1][0], f[0][1][1]));
    f[2][1][1] = trr(tb[1] + 25088);
    o[2][1] = gu2(m0, m1, r2[0]);
    lda<13, 1024>(sws, l16, wdso + 12288);
    SB();
    SB();
    mfma32a<22, false>(acc[1][0][0], cat(f[1][0][0], f[1][0][1]));
    f[0][0][0] = trr(tb[0] + 26624);
    gu1(m0, m1, acc[0][1][0][4], acc[0][1][0][12], acc[0][1][0][5], acc[0][1][0][13], r2[0]);
    SB();
    mfma32a<23, false>(acc[1][1][0], cat(f[1][0][0], f[1][0][1]));
    f[0][0][1] = trr(tb[0] + 27136);
    o[2][2] = gu2(m0, m1, r2[0]);
    lda<14, 2048>(sws, l16, wdso + 12288);
    SB();
    mfma32a<22, false>(acc[1][0][1], cat(f[1][1][0], f[1][1][1]));
    f[0][1][0] = trr(tb[1] + 26624);
    gu1(m0, m1, acc[0][1][0][6], acc[0][1][0][14], acc[0][1][0][7], acc[0][1][0][15], r2[0]);
    SB();
    mfma32a<23, false>(acc[1][1][1], cat(f[1][1][0], f[1][1][1]));
    f[0][1][1] = trr(tb[1] + 27136);
    o[2][3] = gu2(m0, m1, r2[0]);
    SB();
    SB();
    mfma32a<24, false>(acc[1][0][0], cat(f[2][0][0], f[2][0][1]));
    f[1][0][0] = trr(tb[0] + 28672);
    gu1(m0, m1, acc[0][1][1][0], acc[0][1][1][8], acc[0][1][1][1], acc[0][1][1][9], r2[1]);
    SB();
    mfma32a<25, false>(acc[1][1][0], cat(f[2][0][0], f[2][0][1]));
    f[1][0][1] = trr(tb[0] + 29184);
    o[3][0] = gu2(m0, m1, r2[1]);
    lda<15, 3072>(sws, l16, wdso + 12288);
    SB();
    mfma32a<24, false>(acc[1][0][1], cat(f[2][1][0], f[2][1][1]));
    f[1][1][0] = trr(tb[1] + 28672);
    gu1(m0, m1, acc[0][1][1][2], acc[0][1][1][10], acc[0][1][1][3], acc[0][1][1][11], r2[1]);
    SB();
    mfma32a<25, false>(acc[1][1][1], cat(f[2][1][0], f[2][1][1]));
    f[1][1][1] = trr(tb[1] + 29184);
    o[3][1] = gu2(m0, m1, r2[1]);
    SB();
    SB();
    mfma32a<26, false>(acc[1][0][0], cat(f[0][0][0], f[0][0][1]));
    f[2][0][0] = trr(tb[0] + 30720);
    gu1(m0, m1, acc[0][1][1][4], acc[0][1][1][12], acc[0][1][1][5], acc[0][1][1][13], r2[1]);
    SB();
    mfma32a<27, false>(acc[1][1][0], cat(f[0][0][0], f[0][0][1]));
    f[2][0][1] = trr(tb[0] + 31232);
    o[3][2] = gu2(m0, m1, r2[1]);
    lda<16, 0>(sws, l16, wdso + 16384);
    SB();
    mfma32a<26, false>(acc[1][0][1], cat(f[0][1][0], f[0][1][1]));
    f[2][1][0] = trr(tb[1] + 30720);
    gu1(m0, m1, acc[0][1][1][6], acc[0][1][1][14], acc[0][1][1][7], acc[0][1][1][15], r2[1]);
    SB();
    mfma32a<27, false>(acc[1][1][1], cat(f[0][1][0], f[0][1][1]));
    f[2][1][1] = trr(tb[1] + 31232);
    o[3][3] = gu2(m0, m1, r2[1]);
    lda<17, 1024>(sws, l16, wdso + 16384);
    st16c(o[0], rws, l16, hh_off(0, 0));
    st16c(o[1], rws, l16, hh_off(0, 1));
    st16c(o[2], rws, l16, hh_off(0, 2));
    st16c(o[3], rws, l16, hh_off(0, 3));
    SB();
    SB();
    mfma32a<28, false>(acc[1][0][0], cat(f[1][0][0], f[1][0][1]));
    f[0][0][0] = trr(tb[0] + 32768);
    SB();
    mfma32a<29, false>(acc[1][1][0], cat(f[1][0][0], f[1][0][1]));
    f[0][0][1] = trr(tb[0] + 33280);
    lda<18, 2048>(sws, l16, wdso + 16384);
    SB();
    mfma32a<28, false>(acc[1][0][1], cat(f[1][1][0], f[1][1][1]));
    f[0][1][0] = trr(tb[1] + 32768);
    SB();
    mfma32a<29, false>(acc[1][1][1], cat(f[1][1][0], f[1][1][1]));
    f[0][1][1] = trr(tb[1] + 33280);
    SB();
    SB();
    mfma32a<30, false>(acc[1][0][0], cat(f[2][0][0], f[2][0][1]));
    f[1][0][0] = trr(tb[0] + 34816);
    SB();
    mfma32a<31, false>(acc[1][1][0], cat(f[2][0][0], f[2][0][1]));
    f[1][0][1] = trr(tb[0] + 35328);
    lda<19, 3072>(sws, l16, wdso + 16384);
    SB();
    mfma32a<30, false>(acc[1][0][1], cat(f[2][1][0], f[2][1][1]));
    f[1][1][0] = trr(tb[1] + 34816);
    SB();
    mfma32a<31, false>(acc[1][1][1], cat(f[2][1][0], f[2][1][1]));
    f[1][1][1] = trr(tb[1] + 35328);
    SB();
    SB();
    mfma32a<32, false>(acc[1][0][0], cat(f[0][0][0], f[0][0][1]));
    f[2][0][0] = trr(tb[0] + 36864);
    SB();
    mfma32a<33, false>(acc[1][1][0], cat(f[0][0][0], f[0][0][1]));
    f[2][0][1] = trr(tb[0] + 37376);
    lda<20, 0>(sws, l16, wdso + 20480);
    SB();
    mfma32a<32, false>(acc[1][0][1], cat(f[0][1][0], f[0][1][1]));
    f[2][1][0] = trr(tb[1] + 36864);
    SB();
    mfma32a<33, false>(acc[1][1][1], cat(f[0][1][0], f[0][1][1]));
    f[2][1][1] = trr(tb[1] + 37376);
    lda<21, 1024>(sws, l16, wdso + 20480);
    SB();
    SB();
    mfma32a<34, false>(acc[1][0][0], cat(f[1][0][0], f[1][0][1]));
    f[0][0][0] = trr(tb[0] + 38912);
    SB();
    mfma32a<35, false>(acc[1][1][0], cat(f[1][0][0], f[1][0][1]));
    f[0][0][1] = trr(tb[0] + 39424);
    lda<22, 2048>(sws, l16, wdso + 20480);
    SB();
    mfma32a<34, false>(acc[1][0][1], cat(f[1][1][0], f[1][1][1]));
    f[0][1][0] = trr(tb[1] + 38912);
    SB();
    mfma32a<35, false>(acc[1][1][1], cat(f[1][1][0], f[1][1][1]));
    f[0][1][1] = trr(tb[1] + 39424);
    SB();
    SB();
    mfma32a<36, false>(acc[1][0][0], cat(f[2][0][0], f[2][0][1]));
    f[1][0][0] = trr(tb[0] + 40960);
    lda<23, 3072>(sws, l16, wdso + 20480);
    SB();
    mfma32a<37, false>(acc[1][1][0], cat(f[2][0][0], f[2][0][1]));
    f[1][0][1] = trr(tb[0] + 41472);
    ald16o<0>(sb[0], sws, ro, sso + 16384);
    SB();
    mfma32a<36, false>(acc[1][0][1], cat(f[2][1][0], f[2][1][1]));
    f[1][1][0] = trr(tb[1] + 40960);
    SB();
    mfma32a<37, false>(acc[1][1][1], cat(f[2][1][0], f[2][1][1]));
    f[1][1][1] = trr(tb[1] + 41472);
    ald16o<512>(sb[1], sws, ro, sso + 16384);
    SB();
    SB();
    mfma32a<38, false>(acc[1][0][0], cat(f[0][0][0], f[0][0][1]));
    f[2][0][0] = trr(tb[0] + 43008);
    SB();
    mfma32a<39, false>(acc[1][1][0], cat(f[0][0][0], f[0][0][1]));
    f[2][0][1] = trr(tb[0] + 43520);
    lda<24, 0>(sws, l16, wdso + 24576);
    SB();
    mfma32a<38, false>(acc[1][0][1], cat(f[0][1][0], f[0][1][1]));
    f[2][1][0] = trr(tb[1] + 43008);
    SB();
    mfma32a<39, false>(acc[1][1][1], cat(f[0][1][0], f[0][1][1]));
    f[2][1][1] = trr(tb[1] + 43520);
    lda<25, 1024>(sws, l16, wdso + 24576);
    SB();
    PAGE(p2);
    SB();
    mfma32a<40, false>(acc[1][0][0], cat(f[1][0][0], f[1][0][1]));
    f[0][0][0] = trr(tb[0] + 45056);
    SB();
    mfma32a<41, false>(acc[1][1][0], cat(f[1][0][0], f[1][0][1]));
    f[0][0][1] = trr(tb[0] + 45568);
    SB();
    mfma32a<40, false>(acc[1][0][1], cat(f[1][1][0], f[1][1][1]));
    f[0][1][0] = trr(tb[1] + 45056);
    SB();
    mfma32a<41, false>(acc[1][1][1], cat(f[1][1][0], f[1][1][1]));
    f[0][1][1] = trr(tb[1] + 45568);
    SB();
    SB();
    mfma32a<42, false>(acc[1][0][0], cat(f[2][0][0], f[2][0][1]));
    f[1][0][0] = trr(tb[0] + 47104);
    SB();
    mfma32a<43, false>(acc[1][1][0], cat(f[2][0][0], f[2][0][1]));
    f[1][0][1] = trr(tb[0] + 47616);
    SB();
    mfma32a<42, false>(acc[1][0][1], cat(f[2][1][0], f[2][1][1]));
    f[1][1][0] = trr(tb[1] + 47104);
    SB();
    mfma32a<43, false>(acc[1][1][1], cat(f[2][1][0], f[2][1][1]));
    f[1][1][1] = trr(tb[1] + 47616);
    SB();
    SB();
    mfma32a<44, false>(acc[1][0][0], cat(f[0][0][0], f[0][0][1]));
    f[2][0][0] = trr(tb[0] + 49152);
    SB();
    mfma32a<45, false>(acc[1][1][0], cat(f[0][0][0], f[0][0][1]));
    f[2][0][1] = trr(tb[0] + 49664);
    SB();
    mfma32a<44, false>(acc[1][0][1], cat(f[0][1][0], f[0][1][1]));
    f[2][1][0] = trr(tb[1] + 49152);
    SB();
    mfma32a<45, false>(acc[1][1][1], cat(f[0][1][0], f[0][1][1]));
    f[2][1][1] = trr(tb[1] + 49664);
    SB();
    SB();
    mfma32a<46, false>(acc[1][0][0], cat(f[1][0][0], f[1][0][1]));
    f[0][0][0] = trr(tb[0] + 51200);
    SB();
    mfma32a<47, false>(acc[1][1][0], cat(f[1][0][0], f[1][0][1]));
    f[0][0][1] = trr(tb[0] + 51712);
    SB();
    mfma32a<46, false>(acc[1][0][1], cat(f[1][1][0], f[1][1][1]));
    f[0][1][0] = trr(tb[1] + 51200);
    SB();
    mfma32a<47, false>(acc[1][1][1], cat(f[1][1][0], f[1][1][1]));
    f[0][1][1] = trr(tb[1] + 51712);
    SB();
    WAITV(10);
    flag(FLAGC_OFF + (mt * 56 + nt * 4 + w) * FS);
    SB();
    mfma32a<48, false>(acc[1][0][0], cat(f[2][0][0], f[2][0][1]));
    f[1][0][0] = trr(tb[0] + 53248);
    SB();
    mfma32a<49, false>(acc[1][1][0], cat(f[2][0][0], f[2][0][1]));
    f[1][0][1] = trr(tb[0] + 53760);
    SB();
    mfma32a<48, false>(acc[1][0][1], cat(f[2][1][0], f[2][1][1]));
    f[1][1][0] = trr(tb[1] + 53248);
    SB();
    mfma32a<49, false>(acc[1][1][1], cat(f[2][1][0], f[2][1][1]));
    f[1][1][1] = trr(tb[1] + 53760);
    SB();
    SB();
    mfma32a<50, false>(acc[1][0][0], cat(f[0][0][0], f[0][0][1]));
    f[2][0][0] = trr(tb[0] + 55296);
    SB();
    mfma32a<51, false>(acc[1][1][0], cat(f[0][0][0], f[0][0][1]));
    f[2][0][1] = trr(tb[0] + 55808);
    lda<26, 2048>(sws, l16, wdso + 24576);
    SB();
    mfma32a<50, false>(acc[1][0][1], cat(f[0][1][0], f[0][1][1]));
    f[2][1][0] = trr(tb[1] + 55296);
    SB();
    mfma32a<51, false>(acc[1][1][1], cat(f[0][1][0], f[0][1][1]));
    f[2][1][1] = trr(tb[1] + 55808);
    lda<27, 3072>(sws, l16, wdso + 24576);
    SB();
    SB();
    mfma32a<52, false>(acc[1][0][0], cat(f[1][0][0], f[1][0][1]));
    f[0][0][0] = trr(tb[0] + 57344);
    SB();
    mfma32a<53, false>(acc[1][1][0], cat(f[1][0][0], f[1][0][1]));
    f[0][0][1] = trr(tb[0] + 57856);
    lda<28, 0>(sws, l16, wdso + 28672);
    SB();
    mfma32a<52, false>(acc[1][0][1], cat(f[1][1][0], f[1][1][1]));
    f[0][1][0] = trr(tb[1] + 57344);
    SB();
    mfma32a<53, false>(acc[1][1][1], cat(f[1][1][0], f[1][1][1]));
    f[0][1][1] = trr(tb[1] + 57856);
    SB();
    SB();
    mfma32a<54, false>(acc[1][0][0], cat(f[2][0][0], f[2][0][1]));
    f[1][0][0] = trr(tb[0] + 59392);
    SB();
    mfma32a<55, false>(acc[1][1][0], cat(f[2][0][0], f[2][0][1]));
    f[1][0][1] = trr(tb[0] + 59904);
    lda<29, 1024>(sws, l16, wdso + 28672);
    SB();
    mfma32a<54, false>(acc[1][0][1], cat(f[2][1][0], f[2][1][1]));
    f[1][1][0] = trr(tb[1] + 59392);
    SB();
    mfma32a<55, false>(acc[1][1][1], cat(f[2][1][0], f[2][1][1]));
    f[1][1][1] = trr(tb[1] + 59904);
    SB();
    WAITV(7);
    pin(sb[0]); pin(sb[1]);
    SB();
    mfma32a<56, false>(acc[1][0][0], cat(f[0][0][0], f[0][0][1]));
    f[2][0][0] = trr(tb[0] + 61440);
    bad = stale(sb[0], rt, 0); pin(bad);
    SB();
    mfma32a<57, false>(acc[1][1][0], cat(f[0][0][0], f[0][0][1]));
    f[2][0][1] = trr(tb[0] + 61952);
    bad = stale(sb[1], rt, bad); pin(bad);
    lda<30, 2048>(sws, l16, wdso + 28672);
    SB();
    mfma32a<56, false>(acc[1][0][1], cat(f[0][1][0], f[0][1][1]));
    f[2][1][0] = trr(tb[1] + 61440);
    SB();
    mfma32a<57, false>(acc[1][1][1], cat(f[0][1][0], f[0][1][1]));
    f[2][1][1] = trr(tb[1] + 61952);
    again(std::integral_constant<int, 1>{});
    lda<31, 3072>(sws, l16, wdso + 28672);
    SB();
    SB();
    mfma32a<58, false>(acc[1][0][0], cat(f[1][0][0], f[1][0][1]));
    f[0][0][0] = trr(tb[0] + 63488);
    r2[0] = __builtin_amdgcn_rcpf(lo16(sb[0].z)); sb[0].y = 0; pin(r2[0]); pin(sb[0]);
    SB();
    mfma32a<59, false>(acc[1][1][0], cat(f[1][0][0], f[1][0][1]));
    f[0][0][1] = trr(tb[0] + 64000);
    r2[0] *= r2[0]; sb[0].w = 0; pin(r2[0]); pin(sb[0]);
    lda<32, 0>(sws, l16, wdso + 32768);
    SB();
    mfma32a<58, false>(acc[1][0][1], cat(f[1][1][0], f[1][1][1]));
    f[0][1][0] = trr(tb[1] + 63488);
    r2[1] = __builtin_amdgcn_rcpf(lo16(sb[1].z)); sb[1].y = 0; pin(r2[1]); pin(sb[1]);
    SB();
    mfma32a<59, false>(acc[1][1][1], cat(f[1][1][0], f[1][1][1]));
    f[0][1][1] = trr(tb[1] + 64000);
    r2[1] *= r2[1]; sb[1].w = 0; pin(r2[1]); pin(sb[1]);
    SB();
    SB();
    mfma32a<60, false>(acc[1][0][0], cat(f[2][0][0], f[2][0][1]));
    mfma32<false>(acc[1][1][1], ta[1], sb[1]);
    SB();
    mfma32a<61, false>(acc[1][1][0], cat(f[2][0][0], f[2][0][1]));
    mfma32<false>(acc[1][0][1], ta[0], sb[1]);
    lda<33, 1024>(sws, l16, wdso + 32768);
    SB();
    mfma32a<60, false>(acc[1][0][1], cat(f[2][1][0], f[2][1][1]));
    mfma32<false>(acc[1][1][0], ta[1], sb[0]);
    SB();
    mfma32a<61, false>(acc[1][1][1], cat(f[2][1][0], f[2][1][1]));
    mfma32<false>(acc[1][0][0], ta[0], sb[0]);
    SB();
    SB();
    mfma32a<62, false>(acc[1][0][0], cat(f[0][0][0], f[0][0][1]));
    SB();
    mfma32a<63, false>(acc[1][1][0], cat(f[0][0][0], f[0][0][1]));
    lda<34, 2048>(sws, l16, wdso + 32768);
    SB();
    mfma32a<62, false>(acc[1][0][1], cat(f[0][1][0], f[0][1][1]));
    SB();
    mfma32a<63, false>(acc[1][1][1], cat(f[0][1][0], f[0][1][1]));
    lda<35, 3072>(sws, l16, wdso + 32768);
    SB();
    nops(acc[1][0][0]);
    _Pragma("unroll") for (int e = 0; e < 4; ++e) o[0][e] = pk(v2f{acc[1][0][0][2 * e], acc[1][0][0][2 * e + 1]} * v2f{acc[1][0][0][8 + 2 * e], acc[1][0][0][9 + 2 * e]} * r2[0]);
    st16c(o[0], rws, l16, hh_off(1, 0));
    pin(acc[1][1][0]);
    _Pragma("unroll") for (int e = 0; e < 4; ++e) o[0][e] = pk(v2f{acc[1][1][0][2 * e], acc[1][1][0][2 * e + 1]} * v2f{acc[1][1][0][8 + 2 * e], acc[1][1][0][9 + 2 * e]} * r2[0]);
    st16c(o[0], rws, l16, hh_off(1, 2));
    pin(acc[1][0][1]);
    _Pragma("unroll") for (int e = 0; e < 4; ++e) o[0][e] = pk(v2f{acc[1][0][1][2 * e], acc[1][0][1][2 * e + 1]} * v2f{acc[1][0][1][8 + 2 * e], acc[1][0][1][9 + 2 * e]} * r2[1]);
    st16c(o[0], rws, l16, hh_off(1, 1));
    pin(acc[1][1][1]);
    _Pragma("unroll") for (int e = 0; e < 4; ++e) o[0][e] = pk(v2f{acc[1][1][1][2 * e], acc[1][1][1][2 * e + 1]} * v2f{acc[1][1][1][8 + 2 * e], acc[1][1][1][9 + 2 * e]} * r2[1]);
    st16c(o[0], rws, l16, hh_off(1, 3));
    SB();
    peek(fl, fc0);
    check<0>(fl, fc0, tag);
  } else {
    ENTRY(st);
    const int hf = zs & 1, rb = zs >> 1;
    DRAIN(); asm volatile("s_barrier" ::: "memory");
    if (w == 0) add64(sws, lane < 8 ? shard : 0x80000000u, CB);
    lda<22, 2048>(sws, l16, wso + 20480); lda<23, 3072>(sws, l16, wso + 20480); lda<24, 0>(sws, l16, wso + 24576); lda<25, 1024>(sws, l16, wso + 24576); lda<26, 2048>(sws, l16, wso + 24576); lda<27, 3072>(sws, l16, wso + 24576); lda<28, 0>(sws, l16, wso + 28672); lda<29, 1024>(sws, l16, wso + 28672); lda<30, 2048>(sws, l16, wso + 28672); lda<31, 3072>(sws, l16, wso + 28672);
    {
      v16f a[2]; v4u fs[16]; const u32 ad = opq(SIG_L + (lane & 1) * 1024 + h * 16 + hf * 512);
#pragma unroll
      for (int ks = 0; ks < 16; ++ks) fs[ks] = lds16(ad + 32 * ks);
      DRAIN(); SB();
      sfor<0, 16>([&](auto c) { constexpr int ks = c; mfma32b<2 * ks, ks == 0>(a[0], fs[ks]); mfma32b<2 * ks + 1, ks == 0>(a[1], fs[ks]); });
      asm volatile("s_nop 7\n\ts_nop 7" : "+v"(a[0]), "+v"(a[1])::"memory");
      const float t1 = h ? a[1][0] : a[0][0], t2 = h ? a[1][1] : a[0][1];
      v4u rec = {pk(t1, t1 - rbf(t1)), (u32)rt, pk(t2, t2 - rbf(t2)), (u32)(rt >> 32)};
      st16c(rec, rws, su < 56 ? l16 : 0x80000000u, TV_OFF + su * 2048 + hf * 1024); }
    if (w == 0) poll(cnt_addr(CB), full);
    asm volatile("s_barrier" ::: "memory");
    v4u one = {0x3f803f80u, 0x3f803f80u, 0x3f803f80u, 0x3f803f80u}; asm volatile("" : "+v"(one));
#pragma unroll
    for (int n = 0; n < 16; ++n) gldsc(sws, sw64g, X1_OFF + (128 * w + 8 * n) * RS + hf * 2048 + 128 * rb, w * 16384 + n * 1024);
    v16f gs[2], gq[2];
    sfor<0, 4>([&](auto qc) { constexpr int qq = qc; v4s x[2][2][2];
      WAITV(12 - 4 * qq);
#pragma unroll
      for (int k2 = 0; k2 < 2; ++k2)
#pragma unroll
        for (int st = 0; st < 2; ++st)
#pragma unroll
          for (int tt = 0; tt < 2; ++tt) x[k2][st][tt] = trr(opq(w * 16384 + (tr64 ^ (st << 6))) + (2 * qq + k2) * 2048 + tt * 512);
      SB();
      sfor<0, 4>([&](auto c) { constexpr int k2 = c >> 1, st = c & 1; const v4u fr = cat(x[k2][st][0], x[k2][st][1]); mfma32<qq == 0 && k2 == 0>(gq[st], fr, fr); mfma32<qq == 0 && k2 == 0>(gs[st], one, fr); }); });
    asm volatile("s_nop 7\n\ts_nop 7" : "+v"(gs[0]), "+v"(gs[1]), "+v"(gq[0]), "+v"(gq[1])::"memory");
    const int dr = (lane & 3) + 4 * ((lane & 31) >> 3);
#pragma unroll
    for (int st = 0; st < 2; ++st) { float dg = gq[st][0];
#pragma unroll
      for (int r = 1; r < 16; ++r) { const float e = gq[st][r]; dg = dr == r ? e : dg; }
      const float s0 = gs[st][0];
      if (((lane >> 2) & 1) == h) *(LAS v2f*)(RED_L + w * 512 + (32 * st + (lane & 31)) * 8) = v2f{s0, dg}; }
    BAR();
    if (w == 0) { v2f a = {0, 0};
#pragma unroll
      for (int u = 0; u < 4; ++u) a += *(LAS v2f*)(RED_L + u * 512 + lane * 8);
      a *= (1.f / D);
      const float sd = sqrtf(rbf(rbf(fmaxf(a.y - a.x * a.x, 0.f)) + EPS));
      st16c(v4u{pk(-a.x, -a.x), (u32)rt, pk(sd, sd), (u32)(rt >> 32)}, rws, l16, ST2_OFF + hf * 16384 + rb * 1024); }
    res_tile(0); res_tile(1);
    lda<0, 0>(sws, l16, wdso + 0); lda<1, 1024>(sws, l16, wdso + 0); lda<2, 2048>(sws, l16, wdso + 0); lda<3, 3072>(sws, l16, wdso + 0); lda<4, 0>(sws, l16, wdso + 4096); lda<5, 1024>(sws, l16, wdso + 4096); lda<6, 2048>(sws, l16, wdso + 4096); lda<7, 3072>(sws, l16, wdso + 4096); lda<8, 0>(sws, l16, wdso + 8192); lda<9, 1024>(sws, l16, wdso + 8192); lda<10, 2048>(sws, l16, wdso + 8192); lda<11, 3072>(sws, l16, wdso + 8192); lda<12, 0>(sws, l16, wdso + 12288); lda<13, 1024>(sws, l16, wdso + 12288); lda<14, 2048>(sws, l16, wdso + 12288); lda<15, 3072>(sws, l16, wdso + 12288); lda<16, 0>(sws, l16, wdso + 16384); lda<17, 1024>(sws, l16, wdso + 16384); lda<18, 2048>(sws, l16, wdso + 16384); lda<19, 3072>(sws, l16, wdso + 16384); lda<20, 0>(sws, l16, wdso + 20480); lda<21, 1024>(sws, l16, wdso + 20480); lda<22, 2048>(sws, l16, wdso + 20480); lda<23, 3072>(sws, l16, wdso + 20480); lda<24, 0>(sws, l16, wdso + 24576); lda<25, 1024>(sws, l16, wdso + 24576); lda<26, 2048>(sws, l16, wdso + 24576); lda<27, 3072>(sws, l16, wdso + 24576); lda<28, 0>(sws, l16, wdso + 28672); lda<29, 1024>(sws, l16, wdso + 28672); lda<30, 2048>(sws, l16, wdso + 28672); lda<31, 3072>(sws, l16, wdso + 28672); lda<32, 0>(sws, l16, wdso + 32768); lda<33, 1024>(sws, l16, wdso + 32768); lda<34, 2048>(sws, l16, wdso + 32768); lda<35, 3072>(sws, l16, wdso + 32768); lda<36, 0>(sws, l16, wdso + 36864); lda<37, 1024>(sws, l16, wdso + 36864); lda<38, 2048>(sws, l16, wdso + 36864); lda<39, 3072>(sws, l16, wdso + 36864); lda<40, 0>(sws, l16, wdso + 40960); lda<41, 1024>(sws, l16, wdso + 40960); lda<42, 2048>(sws, l16, wdso + 40960); lda<43, 3072>(sws, l16, wdso + 40960); ald16o<0>(wp[0], sws, l16, wdso + 45056); ald16o<1024>(wp[1], sws, l16, wdso + 45056); ald16o<2048>(wp[2], sws, l16, wdso + 45056); ald16o<3072>(wp[3], sws, l16, wdso + 45056); ald16o<0>(wp[4], sws, l16, wdso + 49152); ald16o<1024>(wp[5], sws, l16, wdso + 49152); ald16o<2048>(wp[6], sws, l16, wdso + 49152); ald16o<3072>(wp[7], sws, l16, wdso + 49152); ald16o<0>(wp[8], sws, l16, wdso + 53248); ald16o<1024>(wp[9], sws, l16, wdso + 53248); ald16o<2048>(wp[10], sws, l16, wdso + 53248); ald16o<3072>(wp[11], sws, l16, wdso + 53248);
    poll(fc0, tag);
    asm volatile(".Lmega_end2:");
  }
  PAGE(g2);

  const rsrc_t ro_ = mkrs(P.out, S * D * 2);
  const u32 hso = HH_OFF + (mt2 * 112 + 28 * w) * 1024, oo = (lane & 31) * 1024 + h * 16, oso = 32 * mt2 * 1024 + (64 * dt + 16 * w) * 2;
  const char* fc1 = fc0 + 896 * FS;
  const u32 gba = opq(GB_L + (16 * w + 4 * h) * 4), xra = X1L_L + (16 * w + 4 * h + q4) * 128 + ((q4 >> 1) << 6) + (g & 1) * 32 + p4 * 8;
  const u32 xw = opq(l16 + w * 2048), xr = opq(l16 + w * 8192);
  v4u hv[28], res; v16f y[2][2]; v4f g0, g1, p0[4], p1[4], r0, r1; v2u x0, x1, q0, q1; u64 fl2;
  flag(is_stat ? 0 : FLAGC_OFF + (896 + mt * 56 + nt * 4 + w) * FS, !is_stat);
  ald16o<0>(hv[0], sws, l16, hso + 0);
  ald16o<1024>(hv[1], sws, l16, hso + 0);
  ald16o<2048>(hv[2], sws, l16, hso + 0);
  ald16o<3072>(hv[3], sws, l16, hso + 0);
  ald16o<0>(hv[4], sws, l16, hso + 4096);
  ald16o<1024>(hv[5], sws, l16, hso + 4096);
  ald16o<2048>(hv[6], sws, l16, hso + 4096);
  ald16o<3072>(hv[7], sws, l16, hso + 4096);
  ald16o<0>(hv[8], sws, l16, hso + 8192);
  ald16o<1024>(hv[9], sws, l16, hso + 8192);
  ald16o<2048>(hv[10], sws, l16, hso + 8192);
  ald16o<3072>(hv[11], sws, l16, hso + 8192);
  _Pragma("unroll") for (int k = 0; k < 12; ++k) pin(wp[k]);
  WAITV(11); pin(hv[0]);
  mfma32a<0, true>(y[0][0], hv[0]);
  ald16o<0>(hv[12], sws, l16, hso + 12288);
  mfma32a<1, true>(y[0][1], hv[0]);
  ald16o<1024>(hv[13], sws, l16, hso + 12288);
  SB();
  WAITV(12); pin(hv[1]);
  mfma32a<2, false>(y[0][0], hv[1]);
  ald16o<2048>(hv[14], sws, l16, hso + 12288);
  mfma32a<3, false>(y[0][1], hv[1]);
  ald16o<3072>(hv[15], sws, l16, hso + 12288);
  SB();
  WAITV(13); pin(hv[2]);
  mfma32a<4, false>(y[0][0], hv[2]);
  ald16o<0>(hv[16], sws, l16, hso + 16384);
  mfma32a<5, false>(y[0][1], hv[2]);
  ald16o<1024>(hv[17], sws, l16, hso + 16384);
  SB();
  WAITV(14); pin(hv[3]);
  mfma32a<6, false>(y[0][0], hv[3]);
  ald16o<2048>(hv[22], sws, l16, hso + 20480);
  mfma32a<7, false>(y[0][1], hv[3]);
  ald16o<3072>(hv[23], sws, l16, hso + 20480);
  SB();
  WAITV(15); pin(hv[4]);
  mfma32a<8, false>(y[0][0], hv[4]);
  ald16o<0>(hv[24], sws, l16, hso + 24576);
  mfma32a<9, false>(y[0][1], hv[4]);
  ald16o<1024>(hv[25], sws, l16, hso + 24576);
  SB();
  WAITV(16); pin(hv[5]);
  mfma32a<10, false>(y[0][0], hv[5]);
  ald16o<2048>(hv[26], sws, l16, hso + 24576);
  mfma32a<11, false>(y[0][1], hv[5]);
  ald16o<3072>(hv[27], sws, l16, hso + 24576);
  SB();
  WAITV(17); pin(hv[6]);
  mfma32a<12, false>(y[0][0], hv[6]);
  ald16o<2048>(hv[18], sws, l16, hso + 16384);
  mfma32a<13, false>(y[0][1], hv[6]);
  ald16o<3072>(hv[19], sws, l16, hso + 16384);
  SB();
  WAITV(18); pin(hv[7]);
  mfma32a<14, false>(y[0][0], hv[7]);
  ald16o<0>(hv[20], sws, l16, hso + 20480);
  mfma32a<15, false>(y[0][1], hv[7]);
  ald16o<1024>(hv[21], sws, l16, hso + 20480);
  SB();
  WAITV(19); pin(hv[8]);
  mfma32a<16, false>(y[0][0], hv[8]);
  lda<36, 0>(sws, l16, wdso + 36864);
  mfma32a<17, false>(y[0][1], hv[8]);
  lda<37, 1024>(sws, l16, wdso + 36864);
  SB();
  WAITV(20); pin(hv[9]);
  mfma32a<18, false>(y[0][0], hv[9]);
  lda<38, 2048>(sws, l16, wdso + 36864);
  mfma32a<19, false>(y[0][1], hv[9]);
  lda<39, 3072>(sws, l16, wdso + 36864);
  SB();
  WAITV(21); pin(hv[10]);
  mfma32a<20, false>(y[0][0], hv[10]);
  lda<40, 0>(sws, l16, wdso + 40960);
  mfma32a<21, false>(y[0][1], hv[10]);
  lda<41, 1024>(sws, l16, wdso + 40960);
  SB();
  WAITV(22); pin(hv[11]);
  mfma32a<22, false>(y[0][0], hv[11]);
  lda<42, 2048>(sws, l16, wdso + 40960);
  mfma32a<23, false>(y[0][1], hv[11]);
  lda<43, 3072>(sws, l16, wdso + 40960);
  SB();
  WAITV(23); pin(hv[12]);
  mfma32a<24, false>(y[0][0], hv[12]);
  mfma32a<25, false>(y[0][1], hv[12]);
  SB();
  WAITV(22); pin(hv[13]);
  mfma32a<26, false>(y[0][0], hv[13]);
  mfma32a<27, false>(y[0][1], hv[13]);
  SB();
  WAITV(21); pin(hv[14]);
  mfma32a<28, false>(y[0][0], hv[14]);
  mfma32a<29, false>(y[0][1], hv[14]);
  SB();
  WAITV(20); pin(hv[15]);
  mfma32a<30, false>(y[0][0], hv[15]);
  mfma32a<31, false>(y[0][1], hv[15]);
  SB();
  WAITV(19); pin(hv[16]);
  mfma32a<32, false>(y[0][0], hv[16]);
  mfma32a<33, false>(y[0][1], hv[16]);
  SB();
  WAITV(18); pin(hv[17]);
  mfma32a<34, false>(y[0][0], hv[17]);
  mfma32a<35, false>(y[0][1], hv[17]);
  SB();
  WAITV(17); pin(hv[22]);
  mfma32<false>(y[0][0], wp[0], hv[22]);
  mfma32<false>(y[0][1], wp[1], hv[22]);
  SB();
  WAITV(16); pin(hv[23]);
  mfma32<false>(y[0][0], wp[2], hv[23]);
  mfma32<false>(y[0][1], wp[3], hv[23]);
  SB();
  WAITV(15); pin(hv[24]);
  mfma32<false>(y[0][0], wp[4], hv[24]);
  mfma32<false>(y[0][1], wp[5], hv[24]);
  SB();
  WAITV(14); pin(hv[25]);
  mfma32<false>(y[0][0], wp[6], hv[25]);
  mfma32<false>(y[0][1], wp[7], hv[25]);
  SB();
  WAITV(13); pin(hv[26]);
  mfma32<false>(y[0][0], wp[8], hv[26]);
  mfma32<false>(y[0][1], wp[9], hv[26]);
  SB();
  WAITV(12); pin(hv[27]);
  mfma32<false>(y[0][0], wp[10], hv[27]);
  mfma32<false>(y[0][1], wp[11], hv[27]);
  SB();
  WAITV(6); pin(hv[18]);
  mfma32a<36, false>(y[0][0], hv[18]);
  mfma32a<37, false>(y[0][1], hv[18]);
  SB();
  WAITV(4); pin(hv[19]);
  peek(fl2, fc1);
  mfma32a<38, false>(y[0][0], hv[19]);
  mfma32a<39, false>(y[0][1], hv[19]);
  SB();
  WAITV(3); pin(hv[20]);
  mfma32a<40, false>(y[0][0], hv[20]);
  mfma32a<41, false>(y[0][1], hv[20]);
  SB();
  WAITV(1); pin(hv[21]);
  mfma32a<42, false>(y[0][0], hv[21]);
  mfma32a<43, false>(y[0][1], hv[21]);
  SB();
  asm volatile("s_nop 7\n\ts_nop 3" : "+v"(y[0][0]), "+v"(y[0][1])::"memory");
  sts16(xw + 0, quad<0>(y[0][0]));
  sts16(xw + 1024, quad<4>(y[0][0]));
  sts16(xw + 8192, quad<8>(y[0][0]));
  sts16(xw + 9216, quad<12>(y[0][0]));
  sts16(xw + 16384, quad<0>(y[0][1]));
  sts16(xw + 17408, quad<4>(y[0][1]));
  sts16(xw + 24576, quad<8>(y[0][1]));
  sts16(xw + 25600, quad<12>(y[0][1]));
  g0 = __builtin_bit_cast(v4f, lds16(gba)); g1 = __builtin_bit_cast(v4f, lds16(gba + 32)); SB();
  check<0>(fl2, fc1, tag);
  ald16o<0>(hv[0], sws, l16, hso + 3670016);
  ald16o<1024>(hv[1], sws, l16, hso + 3670016);
  ald16o<2048>(hv[2], sws, l16, hso + 3670016);
  ald16o<3072>(hv[3], sws, l16, hso + 3670016);
  ald16o<0>(hv[4], sws, l16, hso + 3674112);
  ald16o<1024>(hv[5], sws, l16, hso + 3674112);
  ald16o<2048>(hv[6], sws, l16, hso + 3674112);
  ald16o<3072>(hv[7], sws, l16, hso + 3674112);
  ald16o<0>(hv[8], sws, l16, hso + 3678208);
  ald16o<1024>(hv[9], sws, l16, hso + 3678208);
  ald16o<2048>(hv[10], sws, l16, hso + 3678208);
  ald16o<3072>(hv[11], sws, l16, hso + 3678208);
  BAR();
  WAITV(11); pin(hv[0]);
  mfma32a<0, true>(y[1][0], hv[0]);
  ald16o<0>(hv[12], sws, l16, hso + 3682304);
  SB();
  mfma32a<1, true>(y[1][1], hv[0]);
  ald16o<1024>(hv[13], sws, l16, hso + 3682304);
  p0[0] = __builtin_bit_cast(v4f, lds16(xr + 0));
  p1[0] = __builtin_bit_cast(v4f, lds16(xr + 1024));
  SB();
  WAITV(12); pin(hv[1]);
  mfma32a<2, false>(y[1][0], hv[1]);
  ald16o<2048>(hv[14], sws, l16, hso + 3682304);
  SB();
  mfma32a<3, false>(y[1][1], hv[1]);
  ald16o<3072>(hv[15], sws, l16, hso + 3682304);
  p0[1] = __builtin_bit_cast(v4f, lds16(xr + 2048));
  p1[1] = __builtin_bit_cast(v4f, lds16(xr + 3072));
  SB();
  WAITV(13); pin(hv[2]);
  mfma32a<4, false>(y[1][0], hv[2]);
  ald16o<0>(hv[16], sws, l16, hso + 3686400);
  SB();
  mfma32a<5, false>(y[1][1], hv[2]);
  ald16o<1024>(hv[17], sws, l16, hso + 3686400);
  p0[2] = __builtin_bit_cast(v4f, lds16(xr + 4096));
  p1[2] = __builtin_bit_cast(v4f, lds16(xr + 5120));
  SB();
  WAITV(14); pin(hv[3]);
  mfma32a<6, false>(y[1][0], hv[3]);
  ald16o<2048>(hv[18], sws, l16, hso + 3686400);
  SB();
  mfma32a<7, false>(y[1][1], hv[3]);
  ald16o<3072>(hv[19], sws, l16, hso + 3686400);
  p0[3] = __builtin_bit_cast(v4f, lds16(xr + 6144));
  p1[3] = __builtin_bit_cast(v4f, lds16(xr + 7168));
  SB();
  WAITV(15); pin(hv[4]);
  mfma32a<8, false>(y[1][0], hv[4]);
  ald16o<0>(hv[20], sws, l16, hso + 3690496);
  SB();
  mfma32a<9, false>(y[1][1], hv[4]);
  ald16o<1024>(hv[21], sws, l16, hso + 3690496);
  x0 = __builtin_bit_cast(v2u, trr(xra ^ 0)); x1 = __builtin_bit_cast(v2u, trr((xra ^ 0) + 1024));
  SB();
  WAITV(16); pin(hv[5]);
  mfma32a<10, false>(y[1][0], hv[5]);
  ald16o<2048>(hv[22], sws, l16, hso + 3690496);
  SB();
  mfma32a<11, false>(y[1][1], hv[5]);
  ald16o<3072>(hv[23], sws, l16, hso + 3690496);
  r0[0] = sum4(p0[0][0], p0[1][0], p0[2][0], p0[3][0]);
  SB();
  WAITV(17); pin(hv[6]);
  mfma32a<12, false>(y[1][0], hv[6]);
  ald16o<0>(hv[24], sws, l16, hso + 3694592);
  SB();
  mfma32a<13, false>(y[1][1], hv[6]);
  ald16o<1024>(hv[25], sws, l16, hso + 3694592);
  r0[0] = axpy<0>(g0[0], r0[0], x0[0]);
  SB();
  WAITV(18); pin(hv[7]);
  mfma32a<14, false>(y[1][0], hv[7]);
  ald16o<2048>(hv[26], sws, l16, hso + 3694592);
  SB();
  mfma32a<15, false>(y[1][1], hv[7]);
  ald16o<3072>(hv[27], sws, l16, hso + 3694592);
  r0[1] = sum4(p0[0][1], p0[1][1], p0[2][1], p0[3][1]);
  SB();
  WAITV(19); pin(hv[8]);
  mfma32a<16, false>(y[1][0], hv[8]);
  SB();
  mfma32a<17, false>(y[1][1], hv[8]);
  r0[1] = axpy<1>(g0[1], r0[1], x0[0]);
  SB();
  WAITV(18); pin(hv[9]);
  mfma32a<18, false>(y[1][0], hv[9]);
  SB();
  mfma32a<19, false>(y[1][1], hv[9]);
  r0[2] = sum4(p0[0][2], p0[1][2], p0[2][2], p0[3][2]);
  SB();
  WAITV(17); pin(hv[10]);
  mfma32a<20, false>(y[1][0], hv[10]);
  SB();
  mfma32a<21, false>(y[1][1], hv[10]);
  r0[2] = axpy<0>(g0[2], r0[2], x0[1]);
  SB();
  WAITV(16); pin(hv[11]);
  mfma32a<22, false>(y[1][0], hv[11]);
  SB();
  mfma32a<23, false>(y[1][1], hv[11]);
  r0[3] = sum4(p0[0][3], p0[1][3], p0[2][3], p0[3][3]);
  SB();
  WAITV(15); pin(hv[12]);
  mfma32a<24, false>(y[1][0], hv[12]);
  SB();
  mfma32a<25, false>(y[1][1], hv[12]);
  r0[3] = axpy<1>(g0[3], r0[3], x0[1]);
  SB();
  WAITV(14); pin(hv[13]);
  mfma32a<26, false>(y[1][0], hv[13]);
  SB();
  mfma32a<27, false>(y[1][1], hv[13]);
  r1[0] = sum4(p1[0][0], p1[1][0], p1[2][0], p1[3][0]);
  SB();
  WAITV(13); pin(hv[14]);
  mfma32a<28, false>(y[1][0], hv[14]);
  SB();
  mfma32a<29, false>(y[1][1], hv[14]);
  r1[0] = axpy<0>(g1[0], r1[0], x1[0]);
  SB();
  WAITV(12); pin(hv[15]);
  mfma32a<30, false>(y[1][0], hv[15]);
  SB();
  mfma32a<31, false>(y[1][1], hv[15]);
  r1[1] = sum4(p1[0][1], p1[1][1], p1[2][1], p1[3][1]);
  SB();
  WAITV(11); pin(hv[16]);
  mfma32a<32, false>(y[1][0], hv[16]);
  SB();
  mfma32a<33, false>(y[1][1], hv[16]);
  r1[1] = axpy<1>(g1[1], r1[1], x1[0]);
  SB();
  WAITV(10); pin(hv[17]);
  mfma32a<34, false>(y[1][0], hv[17]);
  SB();
  mfma32a<35, false>(y[1][1], hv[17]);
  r1[2] = sum4(p1[0][2], p1[1][2], p1[2][2], p1[3][2]);
  SB();
  WAITV(9); pin(hv[18]);
  mfma32a<36, false>(y[1][0], hv[18]);
  SB();
  mfma32a<37, false>(y[1][1], hv[18]);
  r1[2] = axpy<0>(g1[2], r1[2], x1[1]);
  SB();
  WAITV(8); pin(hv[19]);
  mfma32a<38, false>(y[1][0], hv[19]);
  SB();
  mfma32a<39, false>(y[1][1], hv[19]);
  r1[3] = sum4(p1[0][3], p1[1][3], p1[2][3], p1[3][3]);
  SB();
  WAITV(7); pin(hv[20]);
  mfma32a<40, false>(y[1][0], hv[20]);
  SB();
  mfma32a<41, false>(y[1][1], hv[20]);
  r1[3] = axpy<1>(g1[3], r1[3], x1[1]);
  SB();
  WAITV(6); pin(hv[21]);
  mfma32a<42, false>(y[1][0], hv[21]);
  SB();
  mfma32a<43, false>(y[1][1], hv[21]);
  q0 = v2u{pk(r0.x, r0.y), pk(r0.z, r0.w)}; q1 = v2u{pk(r1.x, r1.y), pk(r1.z, r1.w)}; pin(q0); pin(q1);
  SB();
  WAITV(5); pin(hv[22]);
  mfma32<false>(y[1][0], wp[0], hv[22]);
  SB();
  mfma32<false>(y[1][1], wp[1], hv[22]);
  { auto s0 = __builtin_amdgcn_permlane32_swap(q0.x, q1.x, false, false); auto s1 = __builtin_amdgcn_permlane32_swap(q0.y, q1.y, false, false); res = v4u{(u32)s0[0], (u32)s1[0], (u32)s0[1], (u32)s1[1]}; pin(res); }
  x0 = __builtin_bit_cast(v2u, trr(xra ^ 64)); x1 = __builtin_bit_cast(v2u, trr((xra ^ 64) + 1024));
  SB();
  WAITV(4); pin(hv[23]);
  mfma32<false>(y[1][0], wp[2], hv[23]);
  SB();
  WAITV(3); pin(hv[24]);
  mfma32<false>(y[1][0], wp[4], hv[24]);
  SB();
  WAITV(2); pin(hv[25]);
  mfma32<false>(y[1][0], wp[6], hv[25]);
  SB();
  WAITV(1); pin(hv[26]);
  mfma32<false>(y[1][0], wp[8], hv[26]);
  SB();
  WAITV(0); pin(hv[27]);
  mfma32<false>(y[1][0], wp[10], hv[27]);
  st16(res, ro_, oo, oso); flag(EPOCH_OFF, b == 0 && w == 0);
  SB();
  mfma32<false>(y[1][1], wp[3], hv[23]);
  SB();
  mfma32<false>(y[1][1], wp[5], hv[24]);
  SB();
  mfma32<false>(y[1][1], wp[7], hv[25]);
  SB();
  mfma32<false>(y[1][1], wp[9], hv[26]);
  asm volatile("s_nop 7" : "+v"(y[1][0])::"memory"); sts16(xw + 32768, quad<0>(y[1][0]));
  SB();
  mfma32<false>(y[1][1], wp[11], hv[27]);
  sts16(xw + 33792, quad<4>(y[1][0]));
  SB();
  sts16(xw + 40960, quad<8>(y[1][0]));
  sts16(xw + 41984, quad<12>(y[1][0]));
  asm volatile("s_nop 7\n\ts_nop 3" : "+v"(y[1][1])::"memory");
  sts16(xw + 49152, quad<0>(y[1][1]));
  sts16(xw + 50176, quad<4>(y[1][1]));
  sts16(xw + 57344, quad<8>(y[1][1]));
  sts16(xw + 58368, quad<12>(y[1][1]));
  BAR();
  p0[0] = __builtin_bit_cast(v4f, lds16(xr + 32768)); p1[0] = __builtin_bit_cast(v4f, lds16(xr + 33792));
  p0[1] = __builtin_bit_cast(v4f, lds16(xr + 34816)); p1[1] = __builtin_bit_cast(v4f, lds16(xr + 35840));
  p0[2] = __builtin_bit_cast(v4f, lds16(xr + 36864)); p1[2] = __builtin_bit_cast(v4f, lds16(xr + 37888));
  p0[3] = __builtin_bit_cast(v4f, lds16(xr + 38912)); p1[3] = __builtin_bit_cast(v4f, lds16(xr + 39936));
  r0 = (p0[0] + p0[1]) + (p0[2] + p0[3]); pin(r0);
  r1 = (p1[0] + p1[1]) + (p1[2] + p1[3]); pin(r1);
  q0 = v2u{pk(unpk(x0.x) + v2f{g0.x, g0.y} * v2f{r0.x, r0.y}), pk(unpk(x0.y) + v2f{g0.z, g0.w} * v2f{r0.z, r0.w})}; pin(q0);
  q1 = v2u{pk(unpk(x1.x) + v2f{g1.x, g1.y} * v2f{r1.x, r1.y}), pk(unpk(x1.y) + v2f{g1.z, g1.w} * v2f{r1.z, r1.w})}; pin(q1);
  { auto s0 = __builtin_amdgcn_permlane32_swap(q0.x, q1.x, false, false); auto s1 = __builtin_amdgcn_permlane32_swap(q0.y, q1.y, false, false); res = v4u{(u32)s0[0], (u32)s1[0], (u32)s0[1], (u32)s1[1]}; pin(res); }
  st16(res, ro_, oo, oso + 1024 * 1024);
  asm volatile(".Lmega_end:");
}

extern "C" void kernel_launch(void* const* d_in, const int* in_sizes, int n_in, void* d_out, int out_size, void* d_ws, size_t ws_size, hipStream_t stream) {
  Params p;
  p.q = (const u16*)d_in[0]; p.k = (const u16*)d_in[1]; p.v = (const u16*)d_in[2]; p.sh = (const u16*)d_in[3]; p.sk = (const u16*)d_in[4]; p.Dv = (const u16*)d_in[5];
  p.wg = (const u16*)d_in[7]; p.wu = (const u16*)d_in[8]; p.wd = (const u16*)d_in[9]; p.c = (const u16*)d_in[10]; p.wm = (const u16*)d_in[11];
  p.out = (u16*)d_out; p.ws = (char*)d_ws;
  (void)hipFuncSetAttribute((const void*)mega, hipFuncAttributeMaxDynamicSharedMemorySize, LDS_BYTES);
  mega<<<256, 256, LDS_BYTES, stream>>>(p);
}
```
